```python
import torch

def _softmax(x, axis=-1):
    return torch.softmax(x, dim=axis)
def _gelu(x):
    return torch.nn.functional.gelu(x, approximate="tanh")
def _silu(x):
    return torch.nn.functional.silu(x)
def _sigmoid(x):
    return torch.sigmoid(x)
def _layernorm(x, axis=-1, eps=1e-5):
    m = x.mean(dim=axis, keepdim=True)
    v = x.var(dim=axis, keepdim=True, unbiased=False)
    return (x - m) / torch.sqrt(v + eps)
def _scatter_add(tgt, idx, src, axis=0):
    return tgt.clone().index_add_(axis, idx, src)
def _segment_sum(src, seg, n):
    out = torch.zeros((n,) + src.shape[1:], dtype=src.dtype)
    return out.index_add_(0, seg, src)
def _pad(x, pw):
    flat = tuple(v for pair in reversed(pw) for v in pair)
    return torch.nn.functional.pad(x, flat)

kernel_name = 'standalone__glu_gated_swiglu__prompt'

def setup_inputs():

    _w = (lambda a: (lambda t: t.to(torch.bfloat16) if t.is_floating_point() else t)(torch.as_tensor(a)))
    B, S, D, F = 1, 2048, 1024, 3584
    return {
        "x": _w((lambda *s: torch.randn(*s, dtype=torch.bfloat16) * 0.05)(B, S, D)),
        "w1": _w((lambda *s: torch.randn(*s, dtype=torch.bfloat16) * 0.05)(D, F) * (2.0 / D) ** 0.5),
        "w3": _w((lambda *s: torch.randn(*s, dtype=torch.bfloat16) * 0.05)(D, F) * (2.0 / D) ** 0.5),
        "w2": _w((lambda *s: torch.randn(*s, dtype=torch.bfloat16) * 0.05)(F, D) * (2.0 / F) ** 0.5),
    }

def reference(x, w1, w3, w2):
    gate = torch.einsum("bsd,df->bsf", x, w1)
    up = torch.einsum("bsd,df->bsf", x, w3)
    h = _silu(gate) * up
    out = torch.einsum("bsf,fd->bsd", h, w2)
    return out
```

```cpp
#include <hip/hip_runtime.h>
#include <hip/hip_bf16.h>
typedef unsigned short u16;
__device__ __forceinline__ float bf2f(u16 b){ return __uint_as_float(((unsigned)b)<<16); }
__device__ __forceinline__ u16 f2bf(float f){ __hip_bfloat16 h = __float2bfloat16(f); return *(u16*)&h; }
constexpr int S=2048, D=1024, F=3584;
__global__ void k1(const u16* x, const u16* w1, const u16* w3, u16* h){
  int f = blockIdx.x*256+threadIdx.x; int m = blockIdx.y;
  float g=0,u=0;
  for(int k=0;k<D;++k){ float xv=bf2f(x[m*D+k]); g+=xv*bf2f(w1[k*F+f]); u+=xv*bf2f(w3[k*F+f]); }
  g=bf2f(f2bf(g)); u=bf2f(f2bf(u));
  float s = g/(1.f+__expf(-g)); s=bf2f(f2bf(s));
  h[m*F+f]=f2bf(s*u);
}
__global__ void k2(const u16* h, const u16* w2, u16* out){
  int d = blockIdx.x*256+threadIdx.x; int m = blockIdx.y;
  float a=0;
  for(int k=0;k<F;++k) a+=bf2f(h[m*F+k])*bf2f(w2[k*D+d]);
  out[m*D+d]=f2bf(a);
}
extern "C" void kernel_launch(void* const* d_in, const int* in_sizes, int n_in, void* d_out, int out_size, void* d_ws, size_t ws_size, hipStream_t stream){
  k1<<<dim3(F/256,S),256,0,stream>>>((const u16*)d_in[0],(const u16*)d_in[1],(const u16*)d_in[2],(u16*)d_ws);
  k2<<<dim3(D/256,S),256,0,stream>>>((const u16*)d_ws,(const u16*)d_in[3],(u16*)d_out);
}
```

```cpp
#include <hip/hip_runtime.h>

__global__ __launch_bounds__(256, 1) void swiglu_fused(const char* x, const char* w1, const char* w3, const char* w2, char* ws, char* out) {
  __shared__ __attribute__((aligned(16))) char smem[163840];
  asm volatile(
    "s_load_dwordx2 s[36:37], s[0:1], 0x0\n\t"
    "s_load_dwordx4 s[4:7], s[0:1], 0x8\n\t"
    "s_load_dwordx2 s[48:49], s[0:1], 0x18\n\t"
    "s_load_dwordx2 s[44:45], s[0:1], 0x20\n\t"
    "s_load_dwordx2 s[76:77], s[0:1], 0x28\n\t"
    "s_getpc_b64 s[84:85]\n\t"
    "CP0_%=:\n\t"
    "v_lshlrev_b32 v1, 7, v0\n\t"
    "s_mov_b32 s86, TAIL_%=-CP0_%=\n\t"
    "s_mov_b32 s87, 0x20000\n\t"
    "buffer_load_dword a255, v1, s[84:87], 0 offen\n\t"
    "v_readfirstlane_b32 s3, v0\n\t"
    "v_and_b32 v255, 63, v0\n\t"
    "s_and_b32 s10, s2, 7\n\t"
    "s_bfe_u32 s8, s2, 0x20003\n\t"
    "s_lshr_b32 s9, s2, 5\n\t"
    "s_lshr_b32 s3, s3, 6\n\t"
    "s_and_b32 s61, s2, 1\n\t"
    "s_lshl2_add_u32 s11, s61, s8\n\t"
    "s_lshr_b32 s61, s10, 1\n\t"
    "s_lshl3_add_u32 s33, s61, s9\n\t"
    "s_lshl2_add_u32 s61, s11, s3\n\t"
    "s_lshl_b32 s61, s61, 17\n\t"
    "v_lshrrev_b32 v1, 3, v255\n\t"
    "v_lshrrev_b32 v2, 4, v255\n\t"
    "v_and_b32 v3, 7, v255\n\t"
    "v_xor_b32 v4, v3, v2\n\t"
    "v_lshlrev_b32 v4, 4, v4\n\t"
    "v_lshl_add_u32 v4, v1, 11, v4\n\t"
    "v_add_u32 v224, s61, v4\n\t"
    "v_xor_b32 v4, 64, v224\n\t"
    "v_add_u32 v225, 15360, v4\n\t"
    "v_add_u32 v226, 30720, v224\n\t"
    "v_add_u32 v227, 46080, v4\n\t"
    "v_add_u32 v228, 65536, v224\n\t"
    "v_add_u32 v229, 80896, v4\n\t"
    "v_add_u32 v230, 96256, v224\n\t"
    "v_add_u32 v231, 111616, v4\n\t"
    "s_sub_u32 s61, s8, s9\n\t"
    "s_sub_u32 s61, s61, 1\n\t"
    "s_and_b32 s61, s61, 3\n\t"
    "s_lshl_b32 s61, s61, 2\n\t"
    "s_lshl_b32 s83, 0x000f000f, s61\n\t"
    "s_and_b32 s61, s83, 0x0fffffff\n\t"
    "s_sub_u32 s52, 0x76543210, s83\n\t"
    "s_subb_u32 s53, 0xfedcba98, s61\n\t"
    "s_lshl_b32 s54, s3, 13\n\t"
    "s_mul_i32 s55, s3, 7168\n\t"
    "s_add_u32 s55, s55, 65536\n\t"
    "s_mov_b32 s38, 4194304\n\t"
    "s_mov_b32 s39, 0x20000\n\t"
    "s_mov_b32 s42, 7340032\n\t"
    "s_mov_b32 s43, 0x20000\n\t"
    "s_and_b32 s61, s52, 15\n\t"
    "s_lshr_b64 s[52:53], s[52:53], 4\n\t"
    "s_lshl_b32 s58, s61, 7\n\t"
    "s_mul_i32 s59, s61, 458752\n\t"
    "s_and_b32 s61, s3, 1\n\t"
    "s_mul_i32 s61, s61, 448\n\t"
    "s_mul_i32 s83, s33, 224\n\t"
    "v_add_u32 v5, s61, v255\n\t"
    "s_movk_i32 s31, 0x1c00\n\t"
    "v_mul_u32_u24 v6, 0x124a, v5\n\t"
    "v_lshrrev_b32 v7, 16, v6\n\t"
    "v_lshrrev_b32 v6, 17, v6\n\t"
    "v_and_b32 v6, 4, v6\n\t"
    "v_xor_b32 v6, v6, v7\n\t"
    "v_lshl_add_u32 v8, v5, 4, s83\n\t"
    "v_mad_u32_u24 v8, v6, s31, v8\n\t"
    "v_mul_u32_u24 v7, 0xe0, v7\n\t"
    "v_sub_u32 v232, v8, v7\n\t"
    "v_add_u32 v5, 64, v5\n\t"
    "v_mul_u32_u24 v6, 0x124a, v5\n\t"
    "v_lshrrev_b32 v7, 16, v6\n\t"
    "v_lshrrev_b32 v6, 17, v6\n\t"
    "v_and_b32 v6, 4, v6\n\t"
    "v_xor_b32 v6, v6, v7\n\t"
    "v_lshl_add_u32 v8, v5, 4, s83\n\t"
    "v_mad_u32_u24 v8, v6, s31, v8\n\t"
    "v_mul_u32_u24 v7, 0xe0, v7\n\t"
    "v_sub_u32 v233, v8, v7\n\t"
    "v_subrev_u32 v233, 1024, v233\n\t"
    "v_add_u32 v5, 64, v5\n\t"
    "v_mul_u32_u24 v6, 0x124a, v5\n\t"
    "v_lshrrev_b32 v7, 16, v6\n\t"
    "v_lshrrev_b32 v6, 17, v6\n\t"
    "v_and_b32 v6, 4, v6\n\t"
    "v_xor_b32 v6, v6, v7\n\t"
    "v_lshl_add_u32 v8, v5, 4, s83\n\t"
    "v_mad_u32_u24 v8, v6, s31, v8\n\t"
    "v_mul_u32_u24 v7, 0xe0, v7\n\t"
    "v_sub_u32 v234, v8, v7\n\t"
    "v_subrev_u32 v234, 2048, v234\n\t"
    "v_add_u32 v5, 64, v5\n\t"
    "v_mul_u32_u24 v6, 0x124a, v5\n\t"
    "v_lshrrev_b32 v7, 16, v6\n\t"
    "v_lshrrev_b32 v6, 17, v6\n\t"
    "v_and_b32 v6, 4, v6\n\t"
    "v_xor_b32 v6, v6, v7\n\t"
    "v_lshl_add_u32 v8, v5, 4, s83\n\t"
    "v_mad_u32_u24 v8, v6, s31, v8\n\t"
    "v_mul_u32_u24 v7, 0xe0, v7\n\t"
    "v_sub_u32 v235, v8, v7\n\t"
    "v_subrev_u32 v235, 3072, v235\n\t"
    "v_add_u32 v5, 64, v5\n\t"
    "v_mul_u32_u24 v6, 0x124a, v5\n\t"
    "v_lshrrev_b32 v7, 16, v6\n\t"
    "v_lshrrev_b32 v6, 17, v6\n\t"
    "v_and_b32 v6, 4, v6\n\t"
    "v_xor_b32 v6, v6, v7\n\t"
    "v_lshl_add_u32 v8, v5, 4, s83\n\t"
    "v_mad_u32_u24 v8, v6, s31, v8\n\t"
    "v_mul_u32_u24 v7, 0xe0, v7\n\t"
    "v_sub_u32 v236, v8, v7\n\t"
    "v_add_u32 v5, 64, v5\n\t"
    "v_mul_u32_u24 v6, 0x124a, v5\n\t"
    "v_lshrrev_b32 v7, 16, v6\n\t"
    "v_lshrrev_b32 v6, 17, v6\n\t"
    "v_and_b32 v6, 4, v6\n\t"
    "v_xor_b32 v6, v6, v7\n\t"
    "v_lshl_add_u32 v8, v5, 4, s83\n\t"
    "v_mad_u32_u24 v8, v6, s31, v8\n\t"
    "v_mul_u32_u24 v7, 0xe0, v7\n\t"
    "v_sub_u32 v237, v8, v7\n\t"
    "v_subrev_u32 v237, 1024, v237\n\t"
    "v_add_u32 v5, 64, v5\n\t"
    "v_mul_u32_u24 v6, 0x124a, v5\n\t"
    "v_lshrrev_b32 v7, 16, v6\n\t"
    "v_lshrrev_b32 v6, 17, v6\n\t"
    "v_and_b32 v6, 4, v6\n\t"
    "v_xor_b32 v6, v6, v7\n\t"
    "v_lshl_add_u32 v8, v5, 4, s83\n\t"
    "v_mad_u32_u24 v8, v6, s31, v8\n\t"
    "v_mul_u32_u24 v7, 0xe0, v7\n\t"
    "v_sub_u32 v238, v8, v7\n\t"
    "v_subrev_u32 v238, 2048, v238\n\t"
    "s_cmp_lt_u32 s3, 2\n\t"
    "s_waitcnt lgkmcnt(0)\n\t"
    "s_cselect_b64 s[40:41], s[4:5], s[6:7]\n\t"
    "s_and_b32 s37, s37, 0xffff\n\t"
    "s_and_b32 s41, s41, 0xffff\n\t"
    "s_lshr_b32 s31, s2, 2\n\t"
    "s_xor_b32 s29, s31, s2\n\t"
    "s_bitcmp1_b32 s29, 3\n\t"
    "s_cbranch_scc1 ALT1_%=\n\t"
    "s_mov_b32 m0, s54\n\t"
    "s_nop 0\n\t"
    "buffer_load_dwordx4 v224, s[36:39], s58 offen offset:0 lds\n\t"
    "buffer_load_dwordx4 v225, s[36:39], s58 offen offset:1024 lds\n\t"
    "buffer_load_dwordx4 v226, s[36:39], s58 offen offset:2048 lds\n\t"
    "buffer_load_dwordx4 v227, s[36:39], s58 offen offset:3072 lds\n\t"
    "s_add_u32 m0, s54, 4096\n\t"
    "s_nop 0\n\t"
    "buffer_load_dwordx4 v228, s[36:39], s58 offen offset:0 lds\n\t"
    "buffer_load_dwordx4 v229, s[36:39], s58 offen offset:1024 lds\n\t"
    "buffer_load_dwordx4 v230, s[36:39], s58 offen offset:2048 lds\n\t"
    "buffer_load_dwordx4 v231, s[36:39], s58 offen offset:3072 lds\n\t"
    "s_branch ALT2_%=\n\t"
    "ALT1_%=:\n\t"
    "s_mov_b32 m0, s55\n\t"
    "s_nop 0\n\t"
    "buffer_load_dwordx4 v232, s[40:43], s59 offen offset:0 lds\n\t"
    "buffer_load_dwordx4 v233, s[40:43], s59 offen offset:1024 lds\n\t"
    "buffer_load_dwordx4 v234, s[40:43], s59 offen offset:2048 lds\n\t"
    "buffer_load_dwordx4 v235, s[40:43], s59 offen offset:3072 lds\n\t"
    "s_add_u32 m0, s55, 4096\n\t"
    "s_nop 0\n\t"
    "buffer_load_dwordx4 v236, s[40:43], s59 offen offset:0 lds\n\t"
    "buffer_load_dwordx4 v237, s[40:43], s59 offen offset:1024 lds\n\t"
    "buffer_load_dwordx4 v238, s[40:43], s59 offen offset:2048 lds\n\t"
    "ALT2_%=:\n\t"
    "s_and_b32 s45, s45, 0xffff\n\t"
    "s_mov_b32 s46, 33816576\n\t"
    "s_mov_b32 s47, 0x20000\n\t"
    "s_mov_b32 s83, 33554432\n\t"
    "s_buffer_load_dwordx2 s[74:75], s[44:47], s83\n\t"
    "v_and_b32 v9, 15, v255\n\t"
    "v_lshrrev_b32 v10, 2, v9\n\t"
    "v_and_b32 v11, 3, v255\n\t"
    "v_and_b32 v12, 1, v2\n\t"
    "v_lshl_add_u32 v13, v2, 3, v10\n\t"
    "v_lshl_add_u32 v14, v12, 2, v13\n\t"
    "v_xor_b32 v15, 4, v14\n\t"
    "v_lshlrev_b32 v16, 3, v11\n\t"
    "v_add_u32 v16, 0x10000, v16\n\t"
    "s_movk_i32 s31, 0xe0\n\t"
    "v_mad_u32_u24 v239, v14, s31, v16\n\t"
    "v_mad_u32_u24 v240, v15, s31, v16\n\t"
    "v_mov_b32 v241, v239\n\t"
    "v_mov_b32 v242, v240\n\t"
    "v_lshrrev_b32 v14, 1, v9\n\t"
    "v_xor_b32 v14, v14, v2\n\t"
    "v_lshlrev_b32 v14, 4, v14\n\t"
    "v_lshl_add_u32 v14, v9, 7, v14\n\t"
    "v_add_u32 v243, s54, v14\n\t"
    "v_xor_b32 v244, 64, v243\n\t"
    "s_bitcmp1_b32 s29, 3\n\t"
    "s_cbranch_scc1 ALT3_%=\n\t"
    "s_mov_b32 m0, s55\n\t"
    "s_nop 0\n\t"
    "buffer_load_dwordx4 v232, s[40:43], s59 offen offset:0 lds\n\t"
    "buffer_load_dwordx4 v233, s[40:43], s59 offen offset:1024 lds\n\t"
    "buffer_load_dwordx4 v234, s[40:43], s59 offen offset:2048 lds\n\t"
    "buffer_load_dwordx4 v235, s[40:43], s59 offen offset:3072 lds\n\t"
    "s_add_u32 m0, s55, 4096\n\t"
    "s_nop 0\n\t"
    "buffer_load_dwordx4 v236, s[40:43], s59 offen offset:0 lds\n\t"
    "buffer_load_dwordx4 v237, s[40:43], s59 offen offset:1024 lds\n\t"
    "buffer_load_dwordx4 v238, s[40:43], s59 offen offset:2048 lds\n\t"
    "s_branch ALT4_%=\n\t"
    "ALT3_%=:\n\t"
    "s_mov_b32 m0, s54\n\t"
    "s_nop 0\n\t"
    "buffer_load_dwordx4 v224, s[36:39], s58 offen offset:0 lds\n\t"
    "buffer_load_dwordx4 v225, s[36:39], s58 offen offset:1024 lds\n\t"
    "buffer_load_dwordx4 v226, s[36:39], s58 offen offset:2048 lds\n\t"
    "buffer_load_dwordx4 v227, s[36:39], s58 offen offset:3072 lds\n\t"
    "s_add_u32 m0, s54, 4096\n\t"
    "s_nop 0\n\t"
    "buffer_load_dwordx4 v228, s[36:39], s58 offen offset:0 lds\n\t"
    "buffer_load_dwordx4 v229, s[36:39], s58 offen offset:1024 lds\n\t"
    "buffer_load_dwordx4 v230, s[36:39], s58 offen offset:2048 lds\n\t"
    "buffer_load_dwordx4 v231, s[36:39], s58 offen offset:3072 lds\n\t"
    "ALT4_%=:\n\t"
    "s_mul_i32 s61, s3, 56\n\t"
    "v_add_u32 v14, s61, v255\n\t"
    "v_lshlrev_b32 v14, 7, v14\n\t"
    "v_cmp_gt_u32 vcc, 56, v255\n\t"
    "v_bfrev_b32 v15, 1\n\t"
    "s_nop 0\n\t"
    "v_cndmask_b32 v254, v15, v14, vcc\n\t"
    "s_lshl2_add_u32 s61, s11, s3\n\t"
    "s_lshl_b32 s61, s61, 2\n\t"
    "s_mul_i32 s61, s61, 114816\n\t"
    "v_mul_u32_u24 v14, 114816, v12\n\t"
    "v_lshrrev_b32 v15, 1, v2\n\t"
    "v_lshl_add_u32 v14, v15, 8, v14\n\t"
    "v_lshl_add_u32 v14, v9, 4, v14\n\t"
    "v_add_u32 v245, s61, v14\n\t"
    "s_xor_b32 s9, s9, 1\n\t"
    "s_lshl_b32 s9, s9, 8\n\t"
    "s_lshr_b32 s35, s10, 1\n\t"
    "s_xor_b32 s34, s11, 4\n\t"
    "s_xor_b32 s61, s3, s35\n\t"
    "s_lshr_b32 s61, s61, 1\n\t"
    "s_lshl_b32 s61, s61, 7\n\t"
    "s_mul_i32 s70, s35, 1835008\n\t"
    "s_add_u32 s70, s70, s61\n\t"
    "s_and_b32 s61, s3, 1\n\t"
    "s_mul_i32 s61, s61, 229376\n\t"
    "s_lshl1_add_u32 s30, s61, s70\n\t"
    "s_lshl2_add_u32 s73, s61, s70\n\t"
    "s_add_u32 s73, s73, 196608\n\t"
    "s_add_u32 s70, s70, s61\n\t"
    "s_add_u32 s70, s70, 65536\n\t"
    "s_lshl_b32 s82, s35, 12\n\t"
    "s_and_b32 s61, s35, 1\n\t"
    "s_lshl_b32 s61, s61, 6\n\t"
    "v_lshrrev_b32 v14, 1, v3\n\t"
    "v_xor_b32 v14, v14, v12\n\t"
    "v_lshlrev_b32 v14, 5, v14\n\t"
    "v_xor_b32 v14, s61, v14\n\t"
    "v_and_b32 v15, 1, v255\n\t"
    "v_lshl_add_u32 v14, v15, 4, v14\n\t"
    "v_lshl_add_u32 v14, v1, 11, v14\n\t"
    "v_add_u32 v14, s9, v14\n\t"
    "v_accvgpr_write_b32 a224, v14\n\t"
    "s_lshl_b32 s61, s11, 5\n\t"
    "s_add_u32 s61, s61, s33\n\t"
    "s_lshl_b32 s61, s61, 3\n\t"
    "s_add_u32 s61, s61, 33554688\n\t"
    "v_cmp_eq_u32 vcc, 0, v0\n\t"
    "v_bfrev_b32 v17, 1\n\t"
    "v_mov_b32 v14, s61\n\t"
    "v_cndmask_b32 v14, v17, v14, vcc\n\t"
    "v_accvgpr_write_b32 a225, v14\n\t"
    "s_lshl_b32 s61, s2, 9\n\t"
    "s_add_u32 s61, s61, 33685504\n\t"
    "s_lshl4_add_u32 s61, s3, s61\n\t"
    "v_cmp_eq_u32 vcc, 0, v255\n\t"
    "s_nop 0\n\t"
    "v_mov_b32 v14, s61\n\t"
    "v_cndmask_b32 v15, v17, v14, vcc\n\t"
    "v_accvgpr_write_b32 a231, v15\n\t"
    "v_accvgpr_write_b32 a232, v14\n\t"
    "s_lshl2_add_u32 s61, s34, s35\n\t"
    "s_lshl_b32 s61, s61, 6\n\t"
    "s_add_u32 s61, s61, 33554688\n\t"
    "v_lshl_add_u32 v14, v3, 3, s61\n\t"
    "v_accvgpr_write_b32 a226, v14\n\t"
    "s_lshl2_add_u32 s61, s34, s3\n\t"
    "s_mul_i32 s61, s61, 459264\n\t"
    "v_lshlrev_b32 v14, 4, v255\n\t"
    "v_add_u32 v15, s61, v14\n\t"
    "v_accvgpr_write_b32 a228, v15\n\t"
    "s_lshl_b32 s61, s3, 12\n\t"
    "v_add_u32 v15, s61, v14\n\t"
    "v_accvgpr_write_b32 a229, v15\n\t"
    "s_lshl2_add_u32 s61, s34, s3\n\t"
    "s_lshl_b32 s61, s61, 17\n\t"
    "s_lshl_b32 s83, s35, 6\n\t"
    "s_add_u32 s83, s83, s9\n\t"
    "s_add_u32 s61, s61, s83\n\t"
    "v_lshlrev_b32 v14, 11, v9\n\t"
    "v_lshl_add_u32 v14, v12, 5, v14\n\t"
    "v_lshrrev_b32 v15, 1, v2\n\t"
    "v_lshl_add_u32 v14, v15, 4, v14\n\t"
    "v_add_u32 v14, s61, v14\n\t"
    "v_accvgpr_write_b32 a230, v14\n\t"
    "v_and_b32 v14, 1, v255\n\t"
    "v_lshlrev_b32 v14, 4, v14\n\t"
    "v_mov_b32 v15, 0x3f80\n\t"
    "v_lshlrev_b32 v14, v14, v15\n\t"
    "v_cmp_eq_u32 vcc, v2, v10\n\t"
    "v_and_b32 v15, 2, v255\n\t"
    "s_nop 0\n\t"
    "v_cndmask_b32 v14, 0, v14, vcc\n\t"
    "v_cmp_eq_u32 vcc, 0, v15\n\t"
    "s_nop 1\n\t"
    "v_cndmask_b32 v15, 0, v14, vcc\n\t"
    "v_accvgpr_write_b32 a227, v15\n\t"
    "v_xor_b32 v14, v14, v15\n\t"
    "v_accvgpr_write_b32 a234, v14\n\t"
    "v_lshrrev_b32 v14, 1, v10\n\t"
    "v_lshl_or_b32 v14, v12, 1, v14\n\t"
    "v_lshlrev_b32 v14, 5, v14\n\t"
    "v_lshl_add_u32 v14, v13, 7, v14\n\t"
    "v_lshl_add_u32 v14, v11, 3, v14\n\t"
    "v_accvgpr_write_b32 a233, v14\n\t"
    "s_and_b32 s49, s49, 0xffff\n\t"
    "s_mov_b32 s50, 7340032\n\t"
    "s_mov_b32 s51, 0x20000\n\t"
    "s_and_b32 s77, s77, 0xffff\n\t"
    "s_mov_b32 s78, 4194304\n\t"
    "s_mov_b32 s79, 0x20000\n\t"
    "s_mov_b64 s[64:65], s[48:49]\n\t"
    "s_mov_b32 s66, 0\n\t"
    "s_mov_b32 s67, 0x20000\n\t"
    "s_mul_i32 s68, s2, 28672\n\t"
    "s_lshr_b32 s61, s2, 3\n\t"
    "s_mul_i32 s61, s61, 10\n\t"
    "s_lshr_b32 s61, s61, 5\n\t"
    "s_sub_u32 s69, 9, s61\n\t"
    "s_lshr_b32 s61, s2, 5\n\t"
    "s_sub_u32 s83, s8, s61\n\t"
    "s_add_u32 s83, s83, 2\n\t"
    "s_and_b32 s83, s83, 3\n\t"
    "s_lshl3_add_u32 s61, s61, s10\n\t"
    "s_mul_i32 s68, s83, 64\n\t"
    "s_add_u32 s68, s68, s61\n\t"
    "s_sub_u32 s69, 10, s83\n\t"
    "s_mov_b32 s56, 0\n\t"
    "s_mov_b32 s57, 65536\n\t"
    "s_mov_b32 s60, 14\n\t"
    "s_mov_b32 s62, 0xbfb8aa3b\n\t"
    "s_mov_b32 s63, 0xbfb8aa3b\n\t"
    "s_mov_b32 s72, s54\n\t"
    "s_mul_i32 s61, s2, 28672\n\t"
    "s_add_u32 s90, s48, s61\n\t"
    "s_addc_u32 s91, s49, 0\n\t"
    "s_mov_b32 s98, 0\n\t"
    "s_bfe_u32 s61, s2, 0x10005\n\t"
    "s_lshl_b32 s61, s61, 2\n\t"
    "s_sub_u32 s69, s69, s61\n\t"
    "s_mov_b32 s68, s98\n\t"
    "s_mov_b32 s66, 28672\n\t"
    "s_and_b32 s61, s52, 15\n\t"
    "s_lshr_b64 s[52:53], s[52:53], 4\n\t"
    "s_lshl_b32 s71, s61, 7\n\t"
    "s_mul_i32 s59, s61, 458752\n\t"
    "s_add_u32 m0, s55, 32768\n\t"
    "s_nop 0\n\t"
    "buffer_load_dwordx4 v232, s[40:43], s59 offen offset:0 lds\n\t"
    "buffer_load_dwordx4 v233, s[40:43], s59 offen offset:1024 lds\n\t"
    "buffer_load_dwordx4 v234, s[40:43], s59 offen offset:2048 lds\n\t"
    "buffer_load_dwordx4 v235, s[40:43], s59 offen offset:3072 lds\n\t"
    "s_add_u32 m0, s55, 36864\n\t"
    "s_nop 0\n\t"
    "buffer_load_dwordx4 v236, s[40:43], s59 offen offset:0 lds\n\t"
    "buffer_load_dwordx4 v237, s[40:43], s59 offen offset:1024 lds\n\t"
    "buffer_load_dwordx4 v238, s[40:43], s59 offen offset:2048 lds\n\t"
    "s_and_b32 s61, s52, 15\n\t"
    "s_lshr_b64 s[52:53], s[52:53], 4\n\t"
    "s_lshl_b32 s58, s61, 7\n\t"
    "s_mul_i32 s59, s61, 458752\n\t"
    "s_waitcnt lgkmcnt(0)\n\t"
    "s_add_u32 s74, s74, 1\n\t"
    "s_addc_u32 s75, s75, 0\n\t"
    "s_waitcnt vmcnt(7)\n\t"
    "s_barrier\n\t"
    "ds_read_b128 a[0:3], v243 offset:0\n\t"
    "ds_read_b128 a[16:19], v244 offset:0\n\t"
    "ds_read_b64_tr_b16 a[64:65], v241 offset:0\n\t"
    "ds_read_b64_tr_b16 a[66:67], v242 offset:0\n\t"
    "ds_read_b64_tr_b16 a[68:69], v241 offset:7168\n\t"
    "ds_read_b64_tr_b16 a[70:71], v242 offset:7168\n\t"
    "ds_read_b128 a[4:7], v243 offset:2048\n\t"
    "ds_read_b128 a[20:23], v244 offset:2048\n\t"
    "ds_read_b128 a[8:11], v243 offset:4096\n\t"
    "ds_read_b128 a[24:27], v244 offset:4096\n\t"
    "ds_read_b128 a[12:15], v243 offset:6144\n\t"
    "ds_read_b128 a[28:31], v244 offset:6144\n\t"
    "s_waitcnt lgkmcnt(6)\n\t"
    "ds_read_b64_tr_b16 a[72:73], v241 offset:14336\n\t"
    "ds_read_b64_tr_b16 a[74:75], v242 offset:14336\n\t"
    "ds_read_b64_tr_b16 a[76:77], v241 offset:21504\n\t"
    "ds_read_b64_tr_b16 a[78:79], v242 offset:21504\n\t"
    "PEEL_%=:\n\t"
    "s_waitcnt lgkmcnt(4)\n\t"
    "s_add_u32 s61, s55, s57\n\t"
    "v_mfma_f32_16x16x32_bf16 v[0:3], a[64:67], a[0:3], 0\n\t"
    "ds_read_b64_tr_b16 a[80:81], v241 offset:32\n\t"
    "s_add_u32 m0, s54, 32768\n\t"
    "v_mfma_f32_16x16x32_bf16 v[0:3], a[68:71], a[16:19], v[0:3]\n\t"
    "buffer_load_dwordx4 v224, s[36:39], s71 offen offset:0 lds\n\t"
    "v_mfma_f32_16x16x32_bf16 v[4:7], a[68:71], a[20:23], 0\n\t"
    "ds_read_b64_tr_b16 a[82:83], v242 offset:32\n\t"
    "v_mfma_f32_16x16x32_bf16 v[4:7], a[64:67], a[4:7], v[4:7]\n\t"
    "v_mfma_f32_16x16x32_bf16 v[8:11], a[64:67], a[8:11], 0\n\t"
    "ds_read_b64_tr_b16 a[84:85], v241 offset:7200\n\t"
    "v_mfma_f32_16x16x32_bf16 v[8:11], a[68:71], a[24:27], v[8:11]\n\t"
    "buffer_load_dwordx4 v225, s[36:39], s71 offen offset:1024 lds\n\t"
    "v_mfma_f32_16x16x32_bf16 v[12:15], a[68:71], a[28:31], 0\n\t"
    "ds_read_b64_tr_b16 a[86:87], v242 offset:7200\n\t"
    "v_mfma_f32_16x16x32_bf16 v[12:15], a[64:67], a[12:15], v[12:15]\n\t"
    "s_waitcnt lgkmcnt(4)\n\t"
    "v_mfma_f32_16x16x32_bf16 v[28:31], a[72:75], a[12:15], 0\n\t"
    "ds_read_b64_tr_b16 a[88:89], v241 offset:14368\n\t"
    "v_mfma_f32_16x16x32_bf16 v[28:31], a[76:79], a[28:31], v[28:31]\n\t"
    "buffer_load_dwordx4 v226, s[36:39], s71 offen offset:2048 lds\n\t"
    "v_mfma_f32_16x16x32_bf16 v[24:27], a[76:79], a[24:27], 0\n\t"
    "ds_read_b64_tr_b16 a[90:91], v242 offset:14368\n\t"
    "v_mfma_f32_16x16x32_bf16 v[24:27], a[72:75], a[8:11], v[24:27]\n\t"
    "v_mfma_f32_16x16x32_bf16 v[20:23], a[72:75], a[4:7], 0\n\t"
    "ds_read_b64_tr_b16 a[92:93], v241 offset:21536\n\t"
    "v_mfma_f32_16x16x32_bf16 v[20:23], a[76:79], a[20:23], v[20:23]\n\t"
    "buffer_load_dwordx4 v227, s[36:39], s71 offen offset:3072 lds\n\t"
    "v_mfma_f32_16x16x32_bf16 v[16:19], a[76:79], a[16:19], 0\n\t"
    "ds_read_b64_tr_b16 a[94:95], v242 offset:21536\n\t"
    "v_mfma_f32_16x16x32_bf16 v[16:19], a[72:75], a[0:3], v[16:19]\n\t"
    "s_waitcnt lgkmcnt(4)\n\t"
    "v_mfma_f32_16x16x32_bf16 v[32:35], a[80:83], a[0:3], 0\n\t"
    "ds_read_b64_tr_b16 a[64:65], v241 offset:64\n\t"
    "s_add_u32 m0, s54, 36864\n\t"
    "v_mfma_f32_16x16x32_bf16 v[32:35], a[84:87], a[16:19], v[32:35]\n\t"
    "buffer_load_dwordx4 v228, s[36:39], s71 offen offset:0 lds\n\t"
    "v_mfma_f32_16x16x32_bf16 v[36:39], a[84:87], a[20:23], 0\n\t"
    "ds_read_b64_tr_b16 a[66:67], v242 offset:64\n\t"
    "v_mfma_f32_16x16x32_bf16 v[36:39], a[80:83], a[4:7], v[36:39]\n\t"
    "v_mfma_f32_16x16x32_bf16 v[40:43], a[80:83], a[8:11], 0\n\t"
    "ds_read_b64_tr_b16 a[68:69], v241 offset:7232\n\t"
    "v_mfma_f32_16x16x32_bf16 v[40:43], a[84:87], a[24:27], v[40:43]\n\t"
    "buffer_load_dwordx4 v229, s[36:39], s71 offen offset:1024 lds\n\t"
    "v_mfma_f32_16x16x32_bf16 v[44:47], a[84:87], a[28:31], 0\n\t"
    "ds_read_b64_tr_b16 a[70:71], v242 offset:7232\n\t"
    "v_mfma_f32_16x16x32_bf16 v[44:47], a[80:83], a[12:15], v[44:47]\n\t"
    "s_waitcnt lgkmcnt(4)\n\t"
    "v_mfma_f32_16x16x32_bf16 v[60:63], a[88:91], a[12:15], 0\n\t"
    "ds_read_b64_tr_b16 a[72:73], v241 offset:14400\n\t"
    "v_mfma_f32_16x16x32_bf16 v[60:63], a[92:95], a[28:31], v[60:63]\n\t"
    "buffer_load_dwordx4 v230, s[36:39], s71 offen offset:2048 lds\n\t"
    "v_mfma_f32_16x16x32_bf16 v[56:59], a[92:95], a[24:27], 0\n\t"
    "ds_read_b64_tr_b16 a[74:75], v242 offset:14400\n\t"
    "v_mfma_f32_16x16x32_bf16 v[56:59], a[88:91], a[8:11], v[56:59]\n\t"
    "v_mfma_f32_16x16x32_bf16 v[52:55], a[88:91], a[4:7], 0\n\t"
    "ds_read_b64_tr_b16 a[76:77], v241 offset:21568\n\t"
    "v_mfma_f32_16x16x32_bf16 v[52:55], a[92:95], a[20:23], v[52:55]\n\t"
    "buffer_load_dwordx4 v231, s[36:39], s71 offen offset:3072 lds\n\t"
    "v_mfma_f32_16x16x32_bf16 v[48:51], a[92:95], a[16:19], 0\n\t"
    "ds_read_b64_tr_b16 a[78:79], v242 offset:21568\n\t"
    "v_mfma_f32_16x16x32_bf16 v[48:51], a[88:91], a[0:3], v[48:51]\n\t"
    "s_waitcnt lgkmcnt(4)\n\t"
    "v_mfma_f32_16x16x32_bf16 v[64:67], a[64:67], a[0:3], 0\n\t"
    "ds_read_b64_tr_b16 a[80:81], v241 offset:96\n\t"
    "s_add_u32 m0, s54, 0\n\t"
    "v_mfma_f32_16x16x32_bf16 v[64:67], a[68:71], a[16:19], v[64:67]\n\t"
    "buffer_load_dwordx4 v224, s[36:39], s58 offen offset:0 lds\n\t"
    "v_mfma_f32_16x16x32_bf16 v[68:71], a[68:71], a[20:23], 0\n\t"
    "ds_read_b64_tr_b16 a[82:83], v242 offset:96\n\t"
    "v_mfma_f32_16x16x32_bf16 v[68:71], a[64:67], a[4:7], v[68:71]\n\t"
    "v_mfma_f32_16x16x32_bf16 v[72:75], a[64:67], a[8:11], 0\n\t"
    "ds_read_b64_tr_b16 a[84:85], v241 offset:7264\n\t"
    "v_mfma_f32_16x16x32_bf16 v[72:75], a[68:71], a[24:27], v[72:75]\n\t"
    "buffer_load_dwordx4 v225, s[36:39], s58 offen offset:1024 lds\n\t"
    "v_mfma_f32_16x16x32_bf16 v[76:79], a[68:71], a[28:31], 0\n\t"
    "ds_read_b64_tr_b16 a[86:87], v242 offset:7264\n\t"
    "v_mfma_f32_16x16x32_bf16 v[76:79], a[64:67], a[12:15], v[76:79]\n\t"
    "s_waitcnt lgkmcnt(4)\n\t"
    "v_mfma_f32_16x16x32_bf16 v[92:95], a[72:75], a[12:15], 0\n\t"
    "ds_read_b64_tr_b16 a[88:89], v241 offset:14432\n\t"
    "v_mfma_f32_16x16x32_bf16 v[92:95], a[76:79], a[28:31], v[92:95]\n\t"
    "buffer_load_dwordx4 v226, s[36:39], s58 offen offset:2048 lds\n\t"
    "v_mfma_f32_16x16x32_bf16 v[88:91], a[76:79], a[24:27], 0\n\t"
    "ds_read_b64_tr_b16 a[90:91], v242 offset:14432\n\t"
    "v_mfma_f32_16x16x32_bf16 v[88:91], a[72:75], a[8:11], v[88:91]\n\t"
    "v_mfma_f32_16x16x32_bf16 v[84:87], a[72:75], a[4:7], 0\n\t"
    "ds_read_b64_tr_b16 a[92:93], v241 offset:21600\n\t"
    "v_mfma_f32_16x16x32_bf16 v[84:87], a[76:79], a[20:23], v[84:87]\n\t"
    "buffer_load_dwordx4 v227, s[36:39], s58 offen offset:3072 lds\n\t"
    "v_mfma_f32_16x16x32_bf16 v[80:83], a[76:79], a[16:19], 0\n\t"
    "ds_read_b64_tr_b16 a[94:95], v242 offset:21600\n\t"
    "v_mfma_f32_16x16x32_bf16 v[80:83], a[72:75], a[0:3], v[80:83]\n\t"
    "s_waitcnt lgkmcnt(4)\n\t"
    "v_mfma_f32_16x16x32_bf16 v[96:99], a[80:83], a[0:3], 0\n\t"
    "ds_read_b64_tr_b16 a[64:65], v241 offset:128\n\t"
    "s_add_u32 m0, s54, 4096\n\t"
    "v_mfma_f32_16x16x32_bf16 v[96:99], a[84:87], a[16:19], v[96:99]\n\t"
    "buffer_load_dwordx4 v228, s[36:39], s58 offen offset:0 lds\n\t"
    "v_mfma_f32_16x16x32_bf16 v[100:103], a[84:87], a[20:23], 0\n\t"
    "ds_read_b64_tr_b16 a[66:67], v242 offset:128\n\t"
    "v_mfma_f32_16x16x32_bf16 v[100:103], a[80:83], a[4:7], v[100:103]\n\t"
    "v_mfma_f32_16x16x32_bf16 v[104:107], a[80:83], a[8:11], 0\n\t"
    "ds_read_b64_tr_b16 a[68:69], v241 offset:7296\n\t"
    "v_mfma_f32_16x16x32_bf16 v[104:107], a[84:87], a[24:27], v[104:107]\n\t"
    "buffer_load_dwordx4 v229, s[36:39], s58 offen offset:1024 lds\n\t"
    "v_mfma_f32_16x16x32_bf16 v[108:111], a[84:87], a[28:31], 0\n\t"
    "ds_read_b64_tr_b16 a[70:71], v242 offset:7296\n\t"
    "v_mfma_f32_16x16x32_bf16 v[108:111], a[80:83], a[12:15], v[108:111]\n\t"
    "s_waitcnt lgkmcnt(4)\n\t"
    "v_mfma_f32_16x16x32_bf16 v[124:127], a[88:91], a[12:15], 0\n\t"
    "ds_read_b64_tr_b16 a[72:73], v241 offset:14464\n\t"
    "v_mfma_f32_16x16x32_bf16 v[124:127], a[92:95], a[28:31], v[124:127]\n\t"
    "buffer_load_dwordx4 v230, s[36:39], s58 offen offset:2048 lds\n\t"
    "v_mfma_f32_16x16x32_bf16 v[120:123], a[92:95], a[24:27], 0\n\t"
    "ds_read_b64_tr_b16 a[74:75], v242 offset:14464\n\t"
    "v_mfma_f32_16x16x32_bf16 v[120:123], a[88:91], a[8:11], v[120:123]\n\t"
    "v_mfma_f32_16x16x32_bf16 v[116:119], a[88:91], a[4:7], 0\n\t"
    "ds_read_b64_tr_b16 a[76:77], v241 offset:21632\n\t"
    "v_mfma_f32_16x16x32_bf16 v[116:119], a[92:95], a[20:23], v[116:119]\n\t"
    "buffer_load_dwordx4 v231, s[36:39], s58 offen offset:3072 lds\n\t"
    "v_mfma_f32_16x16x32_bf16 v[112:115], a[92:95], a[16:19], 0\n\t"
    "ds_read_b64_tr_b16 a[78:79], v242 offset:21632\n\t"
    "v_mfma_f32_16x16x32_bf16 v[112:115], a[88:91], a[0:3], v[112:115]\n\t"
    "s_waitcnt lgkmcnt(4)\n\t"
    "v_mfma_f32_16x16x32_bf16 v[128:131], a[64:67], a[0:3], 0\n\t"
    "ds_read_b64_tr_b16 a[80:81], v241 offset:160\n\t"
    "s_add_u32 m0, s61, 0\n\t"
    "v_mfma_f32_16x16x32_bf16 v[128:131], a[68:71], a[16:19], v[128:131]\n\t"
    "buffer_load_dwordx4 v232, s[40:43], s59 offen offset:0 lds\n\t"
    "v_mfma_f32_16x16x32_bf16 v[132:135], a[68:71], a[20:23], 0\n\t"
    "ds_read_b64_tr_b16 a[82:83], v242 offset:160\n\t"
    "v_mfma_f32_16x16x32_bf16 v[132:135], a[64:67], a[4:7], v[132:135]\n\t"
    "v_mfma_f32_16x16x32_bf16 v[136:139], a[64:67], a[8:11], 0\n\t"
    "ds_read_b64_tr_b16 a[84:85], v241 offset:7328\n\t"
    "v_mfma_f32_16x16x32_bf16 v[136:139], a[68:71], a[24:27], v[136:139]\n\t"
    "buffer_load_dwordx4 v233, s[40:43], s59 offen offset:1024 lds\n\t"
    "v_mfma_f32_16x16x32_bf16 v[140:143], a[68:71], a[28:31], 0\n\t"
    "ds_read_b64_tr_b16 a[86:87], v242 offset:7328\n\t"
    "v_mfma_f32_16x16x32_bf16 v[140:143], a[64:67], a[12:15], v[140:143]\n\t"
    "s_waitcnt lgkmcnt(4)\n\t"
    "v_mfma_f32_16x16x32_bf16 v[156:159], a[72:75], a[12:15], 0\n\t"
    "ds_read_b64_tr_b16 a[88:89], v241 offset:14496\n\t"
    "v_mfma_f32_16x16x32_bf16 v[156:159], a[76:79], a[28:31], v[156:159]\n\t"
    "buffer_load_dwordx4 v234, s[40:43], s59 offen offset:2048 lds\n\t"
    "v_mfma_f32_16x16x32_bf16 v[152:155], a[76:79], a[24:27], 0\n\t"
    "ds_read_b64_tr_b16 a[90:91], v242 offset:14496\n\t"
    "v_mfma_f32_16x16x32_bf16 v[152:155], a[72:75], a[8:11], v[152:155]\n\t"
    "v_mfma_f32_16x16x32_bf16 v[148:151], a[72:75], a[4:7], 0\n\t"
    "ds_read_b64_tr_b16 a[92:93], v241 offset:21664\n\t"
    "v_mfma_f32_16x16x32_bf16 v[148:151], a[76:79], a[20:23], v[148:151]\n\t"
    "buffer_load_dwordx4 v235, s[40:43], s59 offen offset:3072 lds\n\t"
    "v_mfma_f32_16x16x32_bf16 v[144:147], a[76:79], a[16:19], 0\n\t"
    "ds_read_b64_tr_b16 a[94:95], v242 offset:21664\n\t"
    "v_mfma_f32_16x16x32_bf16 v[144:147], a[72:75], a[0:3], v[144:147]\n\t"
    "s_waitcnt lgkmcnt(4)\n\t"
    "v_mfma_f32_16x16x32_bf16 v[160:163], a[80:83], a[0:3], 0\n\t"
    "ds_read_b64_tr_b16 a[64:65], v241 offset:192\n\t"
    "s_add_u32 m0, s61, 4096\n\t"
    "v_mfma_f32_16x16x32_bf16 v[160:163], a[84:87], a[16:19], v[160:163]\n\t"
    "ds_read_b64_tr_b16 a[66:67], v242 offset:192\n\t"
    "buffer_load_dwordx4 v236, s[40:43], s59 offen offset:0 lds\n\t"
    "v_mfma_f32_16x16x32_bf16 v[164:167], a[84:87], a[20:23], 0\n\t"
    "ds_read_b64_tr_b16 a[68:69], v241 offset:7360\n\t"
    "v_mfma_f32_16x16x32_bf16 v[164:167], a[80:83], a[4:7], v[164:167]\n\t"
    "ds_read_b64_tr_b16 a[70:71], v242 offset:7360\n\t"
    "v_mfma_f32_16x16x32_bf16 v[168:171], a[80:83], a[8:11], 0\n\t"
    "ds_read_b64_tr_b16 a[72:73], v241 offset:14528\n\t"
    "v_mfma_f32_16x16x32_bf16 v[168:171], a[84:87], a[24:27], v[168:171]\n\t"
    "ds_read_b64_tr_b16 a[74:75], v242 offset:14528\n\t"
    "buffer_load_dwordx4 v237, s[40:43], s59 offen offset:1024 lds\n\t"
    "v_mfma_f32_16x16x32_bf16 v[172:175], a[84:87], a[28:31], 0\n\t"
    "ds_read_b64_tr_b16 a[76:77], v241 offset:21696\n\t"
    "v_mfma_f32_16x16x32_bf16 v[172:175], a[80:83], a[12:15], v[172:175]\n\t"
    "ds_read_b64_tr_b16 a[78:79], v242 offset:21696\n\t"
    "s_waitcnt vmcnt(14)\n\t"
    "s_waitcnt lgkmcnt(8)\n\t"
    "v_mfma_f32_16x16x32_bf16 v[188:191], a[88:91], a[12:15], 0\n\t"
    "ds_read_b128 a[32:35], v243 offset:32768\n\t"
    "v_mfma_f32_16x16x32_bf16 v[188:191], a[92:95], a[28:31], v[188:191]\n\t"
    "buffer_load_dwordx4 v238, s[40:43], s59 offen offset:2048 lds\n\t"
    "ds_read_b128 a[48:51], v244 offset:32768\n\t"
    "v_mfma_f32_16x16x32_bf16 v[184:187], a[92:95], a[24:27], 0\n\t"
    "ds_read_b128 a[36:39], v243 offset:34816\n\t"
    "v_mfma_f32_16x16x32_bf16 v[184:187], a[88:91], a[8:11], v[184:187]\n\t"
    "ds_read_b128 a[52:55], v244 offset:34816\n\t"
    "s_mov_b32 s57, s56\n\t"
    "v_mfma_f32_16x16x32_bf16 v[180:183], a[88:91], a[4:7], 0\n\t"
    "s_add_u32 s56, s56, 32768\n\t"
    "v_mfma_f32_16x16x32_bf16 v[180:183], a[92:95], a[20:23], v[180:183]\n\t"
    "s_cmp_eq_u32 s56, 98304\n\t"
    "v_mfma_f32_16x16x32_bf16 v[176:179], a[92:95], a[16:19], 0\n\t"
    "s_cselect_b32 s56, 0, s56\n\t"
    "v_mfma_f32_16x16x32_bf16 v[176:179], a[88:91], a[0:3], v[176:179]\n\t"
    "s_and_b32 s61, s52, 15\n\t"
    "s_waitcnt lgkmcnt(8)\n\t"
    "v_mfma_f32_16x16x32_bf16 v[192:195], a[64:67], a[0:3], 0\n\t"
    "ds_read_b128 a[40:43], v243 offset:36864\n\t"
    "s_lshr_b64 s[52:53], s[52:53], 4\n\t"
    "v_mfma_f32_16x16x32_bf16 v[192:195], a[68:71], a[16:19], v[192:195]\n\t"
    "ds_read_b128 a[56:59], v244 offset:36864\n\t"
    "s_lshl_b32 s58, s61, 7\n\t"
    "v_mfma_f32_16x16x32_bf16 v[196:199], a[68:71], a[20:23], 0\n\t"
    "s_cmp_eq_u32 s60, s69\n\t"
    "ds_read_b128 a[44:47], v243 offset:38912\n\t"
    "s_mul_i32 s59, s61, 458752\n\t"
    "v_mfma_f32_16x16x32_bf16 v[196:199], a[64:67], a[4:7], v[196:199]\n\t"
    "s_cbranch_scc1 PFBA_%=\n\t"
    "PFRA_%=:\n\t"
    "ds_read_b128 a[60:63], v244 offset:38912\n\t"
    "s_sub_u32 s60, s60, 1\n\t"
    "v_mfma_f32_16x16x32_bf16 v[200:203], a[64:67], a[8:11], 0\n\t"
    "v_mfma_f32_16x16x32_bf16 v[200:203], a[68:71], a[24:27], v[200:203]\n\t"
    "v_add_u32 v241, s56, v239\n\t"
    "v_mfma_f32_16x16x32_bf16 v[204:207], a[68:71], a[28:31], 0\n\t"
    "v_add_u32 v242, s56, v240\n\t"
    "v_mfma_f32_16x16x32_bf16 v[204:207], a[64:67], a[12:15], v[204:207]\n\t"
    "s_waitcnt lgkmcnt(5)\n\t"
    "s_barrier\n\t"
    "v_mfma_f32_16x16x32_bf16 v[220:223], a[72:75], a[12:15], 0\n\t"
    "ds_read_b64_tr_b16 a[80:81], v241 offset:0\n\t"
    "v_mfma_f32_16x16x32_bf16 v[220:223], a[76:79], a[28:31], v[220:223]\n\t"
    "ds_read_b64_tr_b16 a[82:83], v242 offset:0\n\t"
    "v_mfma_f32_16x16x32_bf16 v[216:219], a[76:79], a[24:27], 0\n\t"
    "ds_read_b64_tr_b16 a[84:85], v241 offset:7168\n\t"
    "v_mfma_f32_16x16x32_bf16 v[216:219], a[72:75], a[8:11], v[216:219]\n\t"
    "ds_read_b64_tr_b16 a[86:87], v242 offset:7168\n\t"
    "v_mfma_f32_16x16x32_bf16 v[212:215], a[72:75], a[4:7], 0\n\t"
    "ds_read_b64_tr_b16 a[88:89], v241 offset:14336\n\t"
    "v_mfma_f32_16x16x32_bf16 v[212:215], a[76:79], a[20:23], v[212:215]\n\t"
    "ds_read_b64_tr_b16 a[90:91], v242 offset:14336\n\t"
    "v_mfma_f32_16x16x32_bf16 v[208:211], a[76:79], a[16:19], 0\n\t"
    "ds_read_b64_tr_b16 a[92:93], v241 offset:21504\n\t"
    "v_mfma_f32_16x16x32_bf16 v[208:211], a[72:75], a[0:3], v[208:211]\n\t"
    "ds_read_b64_tr_b16 a[94:95], v242 offset:21504\n\t"
    "s_branch G1ODD_%=\n\t"
    "G1FIX_%=:\n\t"
    "s_getpc_b64 s[84:85]\n\t"
    "CPB_%=:\n\t"
    "s_add_u32 s84, s84, TAIL_%=-CPB_%=\n\t"
    "s_addc_u32 s85, s85, 0\n\t"
    "s_mov_b32 s86, KEND_%=-TAIL_%=\n\t"
    "s_mov_b32 s87, 0x20000\n\t"
    "buffer_load_dword a255, v254, s[84:87], 0 offen\n\t"
    "v_accvgpr_read_b32 v246, a226\n\t"
    "s_mov_b64 exec, 1\n\t"
    "buffer_load_dword a255, v246, s[44:47], 0 offen sc1\n\t"
    "buffer_load_dword a255, v246, s[44:47], 0 offen offset:2048 sc1\n\t"
    "s_mov_b64 exec, -1\n\t"
    "s_branch G1BACK_%=\n\t"
    "PFBA_%=:\n\t"
    "s_mov_b64 s[64:65], s[90:91]\n\t"
    "s_mov_b64 s[90:91], s[92:93]\n\t"
    "s_mov_b64 s[92:93], s[94:95]\n\t"
    "s_mov_b64 s[94:95], s[96:97]\n\t"
    "buffer_load_dword a255, v254, s[64:67], 0 offen\n\t"
    "s_sub_u32 s69, s69, 4\n\t"
    "s_sub_u32 s68, s68, 1\n\t"
    "s_cselect_b32 s69, -1, s69\n\t"
    "s_branch PFRA_%=\n\t"
    "PFBB_%=:\n\t"
    "s_mov_b64 s[64:65], s[90:91]\n\t"
    "s_mov_b64 s[90:91], s[92:93]\n\t"
    "s_mov_b64 s[92:93], s[94:95]\n\t"
    "s_mov_b64 s[94:95], s[96:97]\n\t"
    "buffer_load_dword a255, v254, s[64:67], 0 offen\n\t"
    "s_sub_u32 s69, s69, 4\n\t"
    "s_sub_u32 s68, s68, 1\n\t"
    "s_cselect_b32 s69, -1, s69\n\t"
    "s_branch PFRB_%=\n\t"
    "PFBC_%=:\n\t"
    "s_mov_b64 s[64:65], s[90:91]\n\t"
    "s_mov_b64 s[90:91], s[92:93]\n\t"
    "s_mov_b64 s[92:93], s[94:95]\n\t"
    "s_mov_b64 s[94:95], s[96:97]\n\t"
    "buffer_load_dword a255, v254, s[64:67], 0 offen\n\t"
    "s_sub_u32 s69, s69, 4\n\t"
    "s_sub_u32 s68, s68, 1\n\t"
    "s_cselect_b32 s69, -1, s69\n\t"
    "s_branch PFRC_%=\n\t"
    ".p2align 6\n\t"
    "G1LOOP_%=:\n\t"
    "s_waitcnt lgkmcnt(4)\n\t"
    "s_add_u32 s61, s55, s57\n\t"
    "v_mfma_f32_16x16x32_bf16 v[0:3], a[64:67], a[0:3], v[0:3]\n\t"
    "ds_read_b64_tr_b16 a[80:81], v241 offset:32\n\t"
    "s_add_u32 m0, s54, 0\n\t"
    "v_mfma_f32_16x16x32_bf16 v[0:3], a[68:71], a[16:19], v[0:3]\n\t"
    "buffer_load_dwordx4 v224, s[36:39], s58 offen offset:0 lds\n\t"
    "v_mfma_f32_16x16x32_bf16 v[4:7], a[68:71], a[20:23], v[4:7]\n\t"
    "ds_read_b64_tr_b16 a[82:83], v242 offset:32\n\t"
    "v_mfma_f32_16x16x32_bf16 v[4:7], a[64:67], a[4:7], v[4:7]\n\t"
    "v_mfma_f32_16x16x32_bf16 v[8:11], a[64:67], a[8:11], v[8:11]\n\t"
    "ds_read_b64_tr_b16 a[84:85], v241 offset:7200\n\t"
    "v_mfma_f32_16x16x32_bf16 v[8:11], a[68:71], a[24:27], v[8:11]\n\t"
    "v_mfma_f32_16x16x32_bf16 v[12:15], a[68:71], a[28:31], v[12:15]\n\t"
    "ds_read_b64_tr_b16 a[86:87], v242 offset:7200\n\t"
    "v_mfma_f32_16x16x32_bf16 v[12:15], a[64:67], a[12:15], v[12:15]\n\t"
    "s_waitcnt lgkmcnt(4)\n\t"
    "v_mfma_f32_16x16x32_bf16 v[28:31], a[72:75], a[12:15], v[28:31]\n\t"
    "ds_read_b64_tr_b16 a[88:89], v241 offset:14368\n\t"
    "v_mfma_f32_16x16x32_bf16 v[28:31], a[76:79], a[28:31], v[28:31]\n\t"
    "buffer_load_dwordx4 v225, s[36:39], s58 offen offset:1024 lds\n\t"
    "v_mfma_f32_16x16x32_bf16 v[24:27], a[76:79], a[24:27], v[24:27]\n\t"
    "ds_read_b64_tr_b16 a[90:91], v242 offset:14368\n\t"
    "v_mfma_f32_16x16x32_bf16 v[24:27], a[72:75], a[8:11], v[24:27]\n\t"
    "v_mfma_f32_16x16x32_bf16 v[20:23], a[72:75], a[4:7], v[20:23]\n\t"
    "ds_read_b64_tr_b16 a[92:93], v241 offset:21536\n\t"
    "v_mfma_f32_16x16x32_bf16 v[20:23], a[76:79], a[20:23], v[20:23]\n\t"
    "buffer_load_dwordx4 v226, s[36:39], s58 offen offset:2048 lds\n\t"
    "v_mfma_f32_16x16x32_bf16 v[16:19], a[76:79], a[16:19], v[16:19]\n\t"
    "ds_read_b64_tr_b16 a[94:95], v242 offset:21536\n\t"
    "v_mfma_f32_16x16x32_bf16 v[16:19], a[72:75], a[0:3], v[16:19]\n\t"
    "s_waitcnt lgkmcnt(4)\n\t"
    "v_mfma_f32_16x16x32_bf16 v[32:35], a[80:83], a[0:3], v[32:35]\n\t"
    "ds_read_b64_tr_b16 a[64:65], v241 offset:64\n\t"
    "v_mfma_f32_16x16x32_bf16 v[32:35], a[84:87], a[16:19], v[32:35]\n\t"
    "v_mfma_f32_16x16x32_bf16 v[36:39], a[84:87], a[20:23], v[36:39]\n\t"
    "ds_read_b64_tr_b16 a[66:67], v242 offset:64\n\t"
    "v_mfma_f32_16x16x32_bf16 v[36:39], a[80:83], a[4:7], v[36:39]\n\t"
    "buffer_load_dwordx4 v227, s[36:39], s58 offen offset:3072 lds\n\t"
    "v_mfma_f32_16x16x32_bf16 v[40:43], a[80:83], a[8:11], v[40:43]\n\t"
    "ds_read_b64_tr_b16 a[68:69], v241 offset:7232\n\t"
    "v_mfma_f32_16x16x32_bf16 v[40:43], a[84:87], a[24:27], v[40:43]\n\t"
    "v_mfma_f32_16x16x32_bf16 v[44:47], a[84:87], a[28:31], v[44:47]\n\t"
    "ds_read_b64_tr_b16 a[70:71], v242 offset:7232\n\t"
    "v_mfma_f32_16x16x32_bf16 v[44:47], a[80:83], a[12:15], v[44:47]\n\t"
    "s_waitcnt lgkmcnt(4)\n\t"
    "v_mfma_f32_16x16x32_bf16 v[60:63], a[88:91], a[12:15], v[60:63]\n\t"
    "ds_read_b64_tr_b16 a[72:73], v241 offset:14400\n\t"
    "s_add_u32 m0, s54, 4096\n\t"
    "v_mfma_f32_16x16x32_bf16 v[60:63], a[92:95], a[28:31], v[60:63]\n\t"
    "buffer_load_dwordx4 v228, s[36:39], s58 offen offset:0 lds\n\t"
    "v_mfma_f32_16x16x32_bf16 v[56:59], a[92:95], a[24:27], v[56:59]\n\t"
    "ds_read_b64_tr_b16 a[74:75], v242 offset:14400\n\t"
    "v_mfma_f32_16x16x32_bf16 v[56:59], a[88:91], a[8:11], v[56:59]\n\t"
    "v_mfma_f32_16x16x32_bf16 v[52:55], a[88:91], a[4:7], v[52:55]\n\t"
    "ds_read_b64_tr_b16 a[76:77], v241 offset:21568\n\t"
    "v_mfma_f32_16x16x32_bf16 v[52:55], a[92:95], a[20:23], v[52:55]\n\t"
    "v_mfma_f32_16x16x32_bf16 v[48:51], a[92:95], a[16:19], v[48:51]\n\t"
    "ds_read_b64_tr_b16 a[78:79], v242 offset:21568\n\t"
    "v_mfma_f32_16x16x32_bf16 v[48:51], a[88:91], a[0:3], v[48:51]\n\t"
    "s_waitcnt lgkmcnt(4)\n\t"
    "v_mfma_f32_16x16x32_bf16 v[64:67], a[64:67], a[0:3], v[64:67]\n\t"
    "ds_read_b64_tr_b16 a[80:81], v241 offset:96\n\t"
    "v_mfma_f32_16x16x32_bf16 v[64:67], a[68:71], a[16:19], v[64:67]\n\t"
    "buffer_load_dwordx4 v229, s[36:39], s58 offen offset:1024 lds\n\t"
    "v_mfma_f32_16x16x32_bf16 v[68:71], a[68:71], a[20:23], v[68:71]\n\t"
    "ds_read_b64_tr_b16 a[82:83], v242 offset:96\n\t"
    "v_mfma_f32_16x16x32_bf16 v[68:71], a[64:67], a[4:7], v[68:71]\n\t"
    "v_mfma_f32_16x16x32_bf16 v[72:75], a[64:67], a[8:11], v[72:75]\n\t"
    "ds_read_b64_tr_b16 a[84:85], v241 offset:7264\n\t"
    "v_mfma_f32_16x16x32_bf16 v[72:75], a[68:71], a[24:27], v[72:75]\n\t"
    "buffer_load_dwordx4 v230, s[36:39], s58 offen offset:2048 lds\n\t"
    "v_mfma_f32_16x16x32_bf16 v[76:79], a[68:71], a[28:31], v[76:79]\n\t"
    "ds_read_b64_tr_b16 a[86:87], v242 offset:7264\n\t"
    "v_mfma_f32_16x16x32_bf16 v[76:79], a[64:67], a[12:15], v[76:79]\n\t"
    "s_waitcnt lgkmcnt(4)\n\t"
    "v_mfma_f32_16x16x32_bf16 v[92:95], a[72:75], a[12:15], v[92:95]\n\t"
    "ds_read_b64_tr_b16 a[88:89], v241 offset:14432\n\t"
    "v_mfma_f32_16x16x32_bf16 v[92:95], a[76:79], a[28:31], v[92:95]\n\t"
    "v_mfma_f32_16x16x32_bf16 v[88:91], a[76:79], a[24:27], v[88:91]\n\t"
    "ds_read_b64_tr_b16 a[90:91], v242 offset:14432\n\t"
    "s_waitcnt vmcnt(14)\n\t"
    "v_mfma_f32_16x16x32_bf16 v[88:91], a[72:75], a[8:11], v[88:91]\n\t"
    "ds_read_b128 a[32:35], v243 offset:32768\n\t"
    "v_mfma_f32_16x16x32_bf16 v[84:87], a[72:75], a[4:7], v[84:87]\n\t"
    "ds_read_b64_tr_b16 a[92:93], v241 offset:21600\n\t"
    "v_mfma_f32_16x16x32_bf16 v[84:87], a[76:79], a[20:23], v[84:87]\n\t"
    "buffer_load_dwordx4 v231, s[36:39], s58 offen offset:3072 lds\n\t"
    "v_mfma_f32_16x16x32_bf16 v[80:83], a[76:79], a[16:19], v[80:83]\n\t"
    "ds_read_b64_tr_b16 a[94:95], v242 offset:21600\n\t"
    "v_mfma_f32_16x16x32_bf16 v[80:83], a[72:75], a[0:3], v[80:83]\n\t"
    "s_waitcnt lgkmcnt(5)\n\t"
    "v_mfma_f32_16x16x32_bf16 v[96:99], a[80:83], a[0:3], v[96:99]\n\t"
    "ds_read_b64_tr_b16 a[64:65], v241 offset:128\n\t"
    "s_add_u32 m0, s61, 0\n\t"
    "v_mfma_f32_16x16x32_bf16 v[96:99], a[84:87], a[16:19], v[96:99]\n\t"
    "buffer_load_dwordx4 v232, s[40:43], s59 offen offset:0 lds\n\t"
    "v_mfma_f32_16x16x32_bf16 v[100:103], a[84:87], a[20:23], v[100:103]\n\t"
    "ds_read_b64_tr_b16 a[66:67], v242 offset:128\n\t"
    "v_mfma_f32_16x16x32_bf16 v[100:103], a[80:83], a[4:7], v[100:103]\n\t"
    "ds_read_b128 a[48:51], v244 offset:32768\n\t"
    "v_mfma_f32_16x16x32_bf16 v[104:107], a[80:83], a[8:11], v[104:107]\n\t"
    "ds_read_b64_tr_b16 a[68:69], v241 offset:7296\n\t"
    "v_mfma_f32_16x16x32_bf16 v[104:107], a[84:87], a[24:27], v[104:107]\n\t"
    "v_mfma_f32_16x16x32_bf16 v[108:111], a[84:87], a[28:31], v[108:111]\n\t"
    "ds_read_b64_tr_b16 a[70:71], v242 offset:7296\n\t"
    "v_mfma_f32_16x16x32_bf16 v[108:111], a[80:83], a[12:15], v[108:111]\n\t"
    "s_waitcnt lgkmcnt(5)\n\t"
    "v_mfma_f32_16x16x32_bf16 v[124:127], a[88:91], a[12:15], v[124:127]\n\t"
    "ds_read_b64_tr_b16 a[72:73], v241 offset:14464\n\t"
    "v_mfma_f32_16x16x32_bf16 v[124:127], a[92:95], a[28:31], v[124:127]\n\t"
    "buffer_load_dwordx4 v233, s[40:43], s59 offen offset:1024 lds\n\t"
    "v_mfma_f32_16x16x32_bf16 v[120:123], a[92:95], a[24:27], v[120:123]\n\t"
    "ds_read_b64_tr_b16 a[74:75], v242 offset:14464\n\t"
    "v_mfma_f32_16x16x32_bf16 v[120:123], a[88:91], a[8:11], v[120:123]\n\t"
    "ds_read_b128 a[36:39], v243 offset:34816\n\t"
    "v_mfma_f32_16x16x32_bf16 v[116:119], a[88:91], a[4:7], v[116:119]\n\t"
    "ds_read_b64_tr_b16 a[76:77], v241 offset:21632\n\t"
    "v_mfma_f32_16x16x32_bf16 v[116:119], a[92:95], a[20:23], v[116:119]\n\t"
    "ds_read_b128 a[52:55], v244 offset:34816\n\t"
    "v_mfma_f32_16x16x32_bf16 v[112:115], a[92:95], a[16:19], v[112:115]\n\t"
    "ds_read_b64_tr_b16 a[78:79], v242 offset:21632\n\t"
    "v_mfma_f32_16x16x32_bf16 v[112:115], a[88:91], a[0:3], v[112:115]\n\t"
    "s_waitcnt lgkmcnt(6)\n\t"
    "v_mfma_f32_16x16x32_bf16 v[128:131], a[64:67], a[0:3], v[128:131]\n\t"
    "ds_read_b64_tr_b16 a[80:81], v241 offset:160\n\t"
    "v_mfma_f32_16x16x32_bf16 v[128:131], a[68:71], a[16:19], v[128:131]\n\t"
    "buffer_load_dwordx4 v234, s[40:43], s59 offen offset:2048 lds\n\t"
    "v_mfma_f32_16x16x32_bf16 v[132:135], a[68:71], a[20:23], v[132:135]\n\t"
    "ds_read_b64_tr_b16 a[82:83], v242 offset:160\n\t"
    "v_mfma_f32_16x16x32_bf16 v[132:135], a[64:67], a[4:7], v[132:135]\n\t"
    "ds_read_b128 a[40:43], v243 offset:36864\n\t"
    "v_mfma_f32_16x16x32_bf16 v[136:139], a[64:67], a[8:11], v[136:139]\n\t"
    "ds_read_b64_tr_b16 a[84:85], v241 offset:7328\n\t"
    "v_mfma_f32_16x16x32_bf16 v[136:139], a[68:71], a[24:27], v[136:139]\n\t"
    "ds_read_b128 a[56:59], v244 offset:36864\n\t"
    "v_mfma_f32_16x16x32_bf16 v[140:143], a[68:71], a[28:31], v[140:143]\n\t"
    "ds_read_b64_tr_b16 a[86:87], v242 offset:7328\n\t"
    "v_mfma_f32_16x16x32_bf16 v[140:143], a[64:67], a[12:15], v[140:143]\n\t"
    "buffer_load_dwordx4 v235, s[40:43], s59 offen offset:3072 lds\n\t"
    "s_waitcnt lgkmcnt(6)\n\t"
    "v_mfma_f32_16x16x32_bf16 v[156:159], a[72:75], a[12:15], v[156:159]\n\t"
    "ds_read_b64_tr_b16 a[88:89], v241 offset:14496\n\t"
    "v_mfma_f32_16x16x32_bf16 v[156:159], a[76:79], a[28:31], v[156:159]\n\t"
    "v_mfma_f32_16x16x32_bf16 v[152:155], a[76:79], a[24:27], v[152:155]\n\t"
    "ds_read_b64_tr_b16 a[90:91], v242 offset:14496\n\t"
    "v_mfma_f32_16x16x32_bf16 v[152:155], a[72:75], a[8:11], v[152:155]\n\t"
    "ds_read_b128 a[44:47], v243 offset:38912\n\t"
    "v_mfma_f32_16x16x32_bf16 v[148:151], a[72:75], a[4:7], v[148:151]\n\t"
    "ds_read_b64_tr_b16 a[92:93], v241 offset:21664\n\t"
    "v_mfma_f32_16x16x32_bf16 v[148:151], a[76:79], a[20:23], v[148:151]\n\t"
    "ds_read_b128 a[60:63], v244 offset:38912\n\t"
    "v_mfma_f32_16x16x32_bf16 v[144:147], a[76:79], a[16:19], v[144:147]\n\t"
    "ds_read_b64_tr_b16 a[94:95], v242 offset:21664\n\t"
    "s_add_u32 m0, s61, 4096\n\t"
    "v_mfma_f32_16x16x32_bf16 v[144:147], a[72:75], a[0:3], v[144:147]\n\t"
    "buffer_load_dwordx4 v236, s[40:43], s59 offen offset:0 lds\n\t"
    "s_waitcnt lgkmcnt(6)\n\t"
    "v_mfma_f32_16x16x32_bf16 v[160:163], a[80:83], a[0:3], v[160:163]\n\t"
    "ds_read_b64_tr_b16 a[64:65], v241 offset:192\n\t"
    "v_mfma_f32_16x16x32_bf16 v[160:163], a[84:87], a[16:19], v[160:163]\n\t"
    "ds_read_b64_tr_b16 a[66:67], v242 offset:192\n\t"
    "v_mfma_f32_16x16x32_bf16 v[164:167], a[84:87], a[20:23], v[164:167]\n\t"
    "ds_read_b64_tr_b16 a[68:69], v241 offset:7360\n\t"
    "v_mfma_f32_16x16x32_bf16 v[164:167], a[80:83], a[4:7], v[164:167]\n\t"
    "ds_read_b64_tr_b16 a[70:71], v242 offset:7360\n\t"
    "v_mfma_f32_16x16x32_bf16 v[168:171], a[80:83], a[8:11], v[168:171]\n\t"
    "ds_read_b64_tr_b16 a[72:73], v241 offset:14528\n\t"
    "v_mfma_f32_16x16x32_bf16 v[168:171], a[84:87], a[24:27], v[168:171]\n\t"
    "ds_read_b64_tr_b16 a[74:75], v242 offset:14528\n\t"
    "buffer_load_dwordx4 v237, s[40:43], s59 offen offset:1024 lds\n\t"
    "v_mfma_f32_16x16x32_bf16 v[172:175], a[84:87], a[28:31], v[172:175]\n\t"
    "ds_read_b64_tr_b16 a[76:77], v241 offset:21696\n\t"
    "v_mfma_f32_16x16x32_bf16 v[172:175], a[80:83], a[12:15], v[172:175]\n\t"
    "ds_read_b64_tr_b16 a[78:79], v242 offset:21696\n\t"
    "s_waitcnt lgkmcnt(8)\n\t"
    "v_mfma_f32_16x16x32_bf16 v[188:191], a[88:91], a[12:15], v[188:191]\n\t"
    "v_mfma_f32_16x16x32_bf16 v[188:191], a[92:95], a[28:31], v[188:191]\n\t"
    "v_mfma_f32_16x16x32_bf16 v[184:187], a[92:95], a[24:27], v[184:187]\n\t"
    "v_mfma_f32_16x16x32_bf16 v[184:187], a[88:91], a[8:11], v[184:187]\n\t"
    "buffer_load_dwordx4 v238, s[40:43], s59 offen offset:2048 lds\n\t"
    "s_mov_b32 s57, s56\n\t"
    "v_mfma_f32_16x16x32_bf16 v[180:183], a[88:91], a[4:7], v[180:183]\n\t"
    "s_add_u32 s56, s56, 32768\n\t"
    "v_mfma_f32_16x16x32_bf16 v[180:183], a[92:95], a[20:23], v[180:183]\n\t"
    "s_cmp_eq_u32 s56, 98304\n\t"
    "v_mfma_f32_16x16x32_bf16 v[176:179], a[92:95], a[16:19], v[176:179]\n\t"
    "s_cselect_b32 s56, 0, s56\n\t"
    "v_mfma_f32_16x16x32_bf16 v[176:179], a[88:91], a[0:3], v[176:179]\n\t"
    "s_and_b32 s61, s52, 15\n\t"
    "s_waitcnt lgkmcnt(4)\n\t"
    "v_mfma_f32_16x16x32_bf16 v[192:195], a[64:67], a[0:3], v[192:195]\n\t"
    "s_lshr_b64 s[52:53], s[52:53], 4\n\t"
    "v_mfma_f32_16x16x32_bf16 v[192:195], a[68:71], a[16:19], v[192:195]\n\t"
    "s_lshl_b32 s58, s61, 7\n\t"
    "v_mfma_f32_16x16x32_bf16 v[196:199], a[68:71], a[20:23], v[196:199]\n\t"
    "s_cmp_eq_u32 s60, s69\n\t"
    "s_mul_i32 s59, s61, 458752\n\t"
    "v_mfma_f32_16x16x32_bf16 v[196:199], a[64:67], a[4:7], v[196:199]\n\t"
    "s_cbranch_scc1 PFBB_%=\n\t"
    "PFRB_%=:\n\t"
    "s_sub_u32 s60, s60, 1\n\t"
    "v_mfma_f32_16x16x32_bf16 v[200:203], a[64:67], a[8:11], v[200:203]\n\t"
    "v_mfma_f32_16x16x32_bf16 v[200:203], a[68:71], a[24:27], v[200:203]\n\t"
    "v_add_u32 v241, s56, v239\n\t"
    "v_mfma_f32_16x16x32_bf16 v[204:207], a[68:71], a[28:31], v[204:207]\n\t"
    "v_add_u32 v242, s56, v240\n\t"
    "v_mfma_f32_16x16x32_bf16 v[204:207], a[64:67], a[12:15], v[204:207]\n\t"
    "s_waitcnt vmcnt(15) lgkmcnt(0)\n\t"
    "s_barrier\n\t"
    "v_mfma_f32_16x16x32_bf16 v[220:223], a[72:75], a[12:15], v[220:223]\n\t"
    "ds_read_b64_tr_b16 a[80:81], v241 offset:0\n\t"
    "v_mfma_f32_16x16x32_bf16 v[220:223], a[76:79], a[28:31], v[220:223]\n\t"
    "ds_read_b64_tr_b16 a[82:83], v242 offset:0\n\t"
    "v_mfma_f32_16x16x32_bf16 v[216:219], a[76:79], a[24:27], v[216:219]\n\t"
    "ds_read_b64_tr_b16 a[84:85], v241 offset:7168\n\t"
    "v_mfma_f32_16x16x32_bf16 v[216:219], a[72:75], a[8:11], v[216:219]\n\t"
    "ds_read_b64_tr_b16 a[86:87], v242 offset:7168\n\t"
    "v_mfma_f32_16x16x32_bf16 v[212:215], a[72:75], a[4:7], v[212:215]\n\t"
    "ds_read_b64_tr_b16 a[88:89], v241 offset:14336\n\t"
    "v_mfma_f32_16x16x32_bf16 v[212:215], a[76:79], a[20:23], v[212:215]\n\t"
    "ds_read_b64_tr_b16 a[90:91], v242 offset:14336\n\t"
    "v_mfma_f32_16x16x32_bf16 v[208:211], a[76:79], a[16:19], v[208:211]\n\t"
    "ds_read_b64_tr_b16 a[92:93], v241 offset:21504\n\t"
    "v_mfma_f32_16x16x32_bf16 v[208:211], a[72:75], a[0:3], v[208:211]\n\t"
    "ds_read_b64_tr_b16 a[94:95], v242 offset:21504\n\t"
    "G1ODD_%=:\n\t"
    "s_waitcnt lgkmcnt(4)\n\t"
    "s_add_u32 s61, s55, s57\n\t"
    "v_mfma_f32_16x16x32_bf16 v[0:3], a[80:83], a[32:35], v[0:3]\n\t"
    "ds_read_b64_tr_b16 a[64:65], v241 offset:32\n\t"
    "s_add_u32 m0, s54, 32768\n\t"
    "v_mfma_f32_16x16x32_bf16 v[0:3], a[84:87], a[48:51], v[0:3]\n\t"
    "buffer_load_dwordx4 v224, s[36:39], s58 offen offset:0 lds\n\t"
    "v_mfma_f32_16x16x32_bf16 v[4:7], a[84:87], a[52:55], v[4:7]\n\t"
    "ds_read_b64_tr_b16 a[66:67], v242 offset:32\n\t"
    "v_mfma_f32_16x16x32_bf16 v[4:7], a[80:83], a[36:39], v[4:7]\n\t"
    "v_mfma_f32_16x16x32_bf16 v[8:11], a[80:83], a[40:43], v[8:11]\n\t"
    "ds_read_b64_tr_b16 a[68:69], v241 offset:7200\n\t"
    "v_mfma_f32_16x16x32_bf16 v[8:11], a[84:87], a[56:59], v[8:11]\n\t"
    "v_mfma_f32_16x16x32_bf16 v[12:15], a[84:87], a[60:63], v[12:15]\n\t"
    "ds_read_b64_tr_b16 a[70:71], v242 offset:7200\n\t"
    "v_mfma_f32_16x16x32_bf16 v[12:15], a[80:83], a[44:47], v[12:15]\n\t"
    "s_waitcnt lgkmcnt(4)\n\t"
    "v_mfma_f32_16x16x32_bf16 v[28:31], a[88:91], a[44:47], v[28:31]\n\t"
    "ds_read_b64_tr_b16 a[72:73], v241 offset:14368\n\t"
    "v_mfma_f32_16x16x32_bf16 v[28:31], a[92:95], a[60:63], v[28:31]\n\t"
    "buffer_load_dwordx4 v225, s[36:39], s58 offen offset:1024 lds\n\t"
    "v_mfma_f32_16x16x32_bf16 v[24:27], a[92:95], a[56:59], v[24:27]\n\t"
    "ds_read_b64_tr_b16 a[74:75], v242 offset:14368\n\t"
    "v_mfma_f32_16x16x32_bf16 v[24:27], a[88:91], a[40:43], v[24:27]\n\t"
    "v_mfma_f32_16x16x32_bf16 v[20:23], a[88:91], a[36:39], v[20:23]\n\t"
    "ds_read_b64_tr_b16 a[76:77], v241 offset:21536\n\t"
    "v_mfma_f32_16x16x32_bf16 v[20:23], a[92:95], a[52:55], v[20:23]\n\t"
    "buffer_load_dwordx4 v226, s[36:39], s58 offen offset:2048 lds\n\t"
    "v_mfma_f32_16x16x32_bf16 v[16:19], a[92:95], a[48:51], v[16:19]\n\t"
    "ds_read_b64_tr_b16 a[78:79], v242 offset:21536\n\t"
    "v_mfma_f32_16x16x32_bf16 v[16:19], a[88:91], a[32:35], v[16:19]\n\t"
    "s_waitcnt lgkmcnt(4)\n\t"
    "v_mfma_f32_16x16x32_bf16 v[32:35], a[64:67], a[32:35], v[32:35]\n\t"
    "ds_read_b64_tr_b16 a[80:81], v241 offset:64\n\t"
    "v_mfma_f32_16x16x32_bf16 v[32:35], a[68:71], a[48:51], v[32:35]\n\t"
    "v_mfma_f32_16x16x32_bf16 v[36:39], a[68:71], a[52:55], v[36:39]\n\t"
    "ds_read_b64_tr_b16 a[82:83], v242 offset:64\n\t"
    "v_mfma_f32_16x16x32_bf16 v[36:39], a[64:67], a[36:39], v[36:39]\n\t"
    "buffer_load_dwordx4 v227, s[36:39], s58 offen offset:3072 lds\n\t"
    "v_mfma_f32_16x16x32_bf16 v[40:43], a[64:67], a[40:43], v[40:43]\n\t"
    "ds_read_b64_tr_b16 a[84:85], v241 offset:7232\n\t"
    "v_mfma_f32_16x16x32_bf16 v[40:43], a[68:71], a[56:59], v[40:43]\n\t"
    "v_mfma_f32_16x16x32_bf16 v[44:47], a[68:71], a[60:63], v[44:47]\n\t"
    "ds_read_b64_tr_b16 a[86:87], v242 offset:7232\n\t"
    "v_mfma_f32_16x16x32_bf16 v[44:47], a[64:67], a[44:47], v[44:47]\n\t"
    "s_waitcnt lgkmcnt(4)\n\t"
    "v_mfma_f32_16x16x32_bf16 v[60:63], a[72:75], a[44:47], v[60:63]\n\t"
    "ds_read_b64_tr_b16 a[88:89], v241 offset:14400\n\t"
    "s_add_u32 m0, s54, 36864\n\t"
    "v_mfma_f32_16x16x32_bf16 v[60:63], a[76:79], a[60:63], v[60:63]\n\t"
    "buffer_load_dwordx4 v228, s[36:39], s58 offen offset:0 lds\n\t"
    "v_mfma_f32_16x16x32_bf16 v[56:59], a[76:79], a[56:59], v[56:59]\n\t"
    "ds_read_b64_tr_b16 a[90:91], v242 offset:14400\n\t"
    "v_mfma_f32_16x16x32_bf16 v[56:59], a[72:75], a[40:43], v[56:59]\n\t"
    "v_mfma_f32_16x16x32_bf16 v[52:55], a[72:75], a[36:39], v[52:55]\n\t"
    "ds_read_b64_tr_b16 a[92:93], v241 offset:21568\n\t"
    "v_mfma_f32_16x16x32_bf16 v[52:55], a[76:79], a[52:55], v[52:55]\n\t"
    "v_mfma_f32_16x16x32_bf16 v[48:51], a[76:79], a[48:51], v[48:51]\n\t"
    "ds_read_b64_tr_b16 a[94:95], v242 offset:21568\n\t"
    "v_mfma_f32_16x16x32_bf16 v[48:51], a[72:75], a[32:35], v[48:51]\n\t"
    "s_waitcnt lgkmcnt(4)\n\t"
    "v_mfma_f32_16x16x32_bf16 v[64:67], a[80:83], a[32:35], v[64:67]\n\t"
    "ds_read_b64_tr_b16 a[64:65], v241 offset:96\n\t"
    "v_mfma_f32_16x16x32_bf16 v[64:67], a[84:87], a[48:51], v[64:67]\n\t"
    "buffer_load_dwordx4 v229, s[36:39], s58 offen offset:1024 lds\n\t"
    "v_mfma_f32_16x16x32_bf16 v[68:71], a[84:87], a[52:55], v[68:71]\n\t"
    "ds_read_b64_tr_b16 a[66:67], v242 offset:96\n\t"
    "v_mfma_f32_16x16x32_bf16 v[68:71], a[80:83], a[36:39], v[68:71]\n\t"
    "v_mfma_f32_16x16x32_bf16 v[72:75], a[80:83], a[40:43], v[72:75]\n\t"
    "ds_read_b64_tr_b16 a[68:69], v241 offset:7264\n\t"
    "v_mfma_f32_16x16x32_bf16 v[72:75], a[84:87], a[56:59], v[72:75]\n\t"
    "buffer_load_dwordx4 v230, s[36:39], s58 offen offset:2048 lds\n\t"
    "v_mfma_f32_16x16x32_bf16 v[76:79], a[84:87], a[60:63], v[76:79]\n\t"
    "ds_read_b64_tr_b16 a[70:71], v242 offset:7264\n\t"
    "v_mfma_f32_16x16x32_bf16 v[76:79], a[80:83], a[44:47], v[76:79]\n\t"
    "s_waitcnt lgkmcnt(4)\n\t"
    "v_mfma_f32_16x16x32_bf16 v[92:95], a[88:91], a[44:47], v[92:95]\n\t"
    "ds_read_b64_tr_b16 a[72:73], v241 offset:14432\n\t"
    "v_mfma_f32_16x16x32_bf16 v[92:95], a[92:95], a[60:63], v[92:95]\n\t"
    "v_mfma_f32_16x16x32_bf16 v[88:91], a[92:95], a[56:59], v[88:91]\n\t"
    "ds_read_b64_tr_b16 a[74:75], v242 offset:14432\n\t"
    "s_waitcnt vmcnt(14)\n\t"
    "v_mfma_f32_16x16x32_bf16 v[88:91], a[88:91], a[40:43], v[88:91]\n\t"
    "ds_read_b128 a[0:3], v243 offset:0\n\t"
    "v_mfma_f32_16x16x32_bf16 v[84:87], a[88:91], a[36:39], v[84:87]\n\t"
    "ds_read_b64_tr_b16 a[76:77], v241 offset:21600\n\t"
    "v_mfma_f32_16x16x32_bf16 v[84:87], a[92:95], a[52:55], v[84:87]\n\t"
    "buffer_load_dwordx4 v231, s[36:39], s58 offen offset:3072 lds\n\t"
    "v_mfma_f32_16x16x32_bf16 v[80:83], a[92:95], a[48:51], v[80:83]\n\t"
    "ds_read_b64_tr_b16 a[78:79], v242 offset:21600\n\t"
    "v_mfma_f32_16x16x32_bf16 v[80:83], a[88:91], a[32:35], v[80:83]\n\t"
    "s_waitcnt lgkmcnt(5)\n\t"
    "v_mfma_f32_16x16x32_bf16 v[96:99], a[64:67], a[32:35], v[96:99]\n\t"
    "ds_read_b64_tr_b16 a[80:81], v241 offset:128\n\t"
    "s_add_u32 m0, s61, 0\n\t"
    "v_mfma_f32_16x16x32_bf16 v[96:99], a[68:71], a[48:51], v[96:99]\n\t"
    "buffer_load_dwordx4 v232, s[40:43], s59 offen offset:0 lds\n\t"
    "v_mfma_f32_16x16x32_bf16 v[100:103], a[68:71], a[52:55], v[100:103]\n\t"
    "ds_read_b64_tr_b16 a[82:83], v242 offset:128\n\t"
    "v_mfma_f32_16x16x32_bf16 v[100:103], a[64:67], a[36:39], v[100:103]\n\t"
    "ds_read_b128 a[16:19], v244 offset:0\n\t"
    "v_mfma_f32_16x16x32_bf16 v[104:107], a[64:67], a[40:43], v[104:107]\n\t"
    "ds_read_b64_tr_b16 a[84:85], v241 offset:7296\n\t"
    "v_mfma_f32_16x16x32_bf16 v[104:107], a[68:71], a[56:59], v[104:107]\n\t"
    "v_mfma_f32_16x16x32_bf16 v[108:111], a[68:71], a[60:63], v[108:111]\n\t"
    "ds_read_b64_tr_b16 a[86:87], v242 offset:7296\n\t"
    "v_mfma_f32_16x16x32_bf16 v[108:111], a[64:67], a[44:47], v[108:111]\n\t"
    "s_waitcnt lgkmcnt(5)\n\t"
    "v_mfma_f32_16x16x32_bf16 v[124:127], a[72:75], a[44:47], v[124:127]\n\t"
    "ds_read_b64_tr_b16 a[88:89], v241 offset:14464\n\t"
    "v_mfma_f32_16x16x32_bf16 v[124:127], a[76:79], a[60:63], v[124:127]\n\t"
    "buffer_load_dwordx4 v233, s[40:43], s59 offen offset:1024 lds\n\t"
    "v_mfma_f32_16x16x32_bf16 v[120:123], a[76:79], a[56:59], v[120:123]\n\t"
    "ds_read_b64_tr_b16 a[90:91], v242 offset:14464\n\t"
    "v_mfma_f32_16x16x32_bf16 v[120:123], a[72:75], a[40:43], v[120:123]\n\t"
    "ds_read_b128 a[4:7], v243 offset:2048\n\t"
    "v_mfma_f32_16x16x32_bf16 v[116:119], a[72:75], a[36:39], v[116:119]\n\t"
    "ds_read_b64_tr_b16 a[92:93], v241 offset:21632\n\t"
    "v_mfma_f32_16x16x32_bf16 v[116:119], a[76:79], a[52:55], v[116:119]\n\t"
    "ds_read_b128 a[20:23], v244 offset:2048\n\t"
    "v_mfma_f32_16x16x32_bf16 v[112:115], a[76:79], a[48:51], v[112:115]\n\t"
    "ds_read_b64_tr_b16 a[94:95], v242 offset:21632\n\t"
    "v_mfma_f32_16x16x32_bf16 v[112:115], a[72:75], a[32:35], v[112:115]\n\t"
    "s_waitcnt lgkmcnt(6)\n\t"
    "v_mfma_f32_16x16x32_bf16 v[128:131], a[80:83], a[32:35], v[128:131]\n\t"
    "ds_read_b64_tr_b16 a[64:65], v241 offset:160\n\t"
    "v_mfma_f32_16x16x32_bf16 v[128:131], a[84:87], a[48:51], v[128:131]\n\t"
    "buffer_load_dwordx4 v234, s[40:43], s59 offen offset:2048 lds\n\t"
    "v_mfma_f32_16x16x32_bf16 v[132:135], a[84:87], a[52:55], v[132:135]\n\t"
    "ds_read_b64_tr_b16 a[66:67], v242 offset:160\n\t"
    "v_mfma_f32_16x16x32_bf16 v[132:135], a[80:83], a[36:39], v[132:135]\n\t"
    "ds_read_b128 a[8:11], v243 offset:4096\n\t"
    "v_mfma_f32_16x16x32_bf16 v[136:139], a[80:83], a[40:43], v[136:139]\n\t"
    "ds_read_b64_tr_b16 a[68:69], v241 offset:7328\n\t"
    "v_mfma_f32_16x16x32_bf16 v[136:139], a[84:87], a[56:59], v[136:139]\n\t"
    "ds_read_b128 a[24:27], v244 offset:4096\n\t"
    "v_mfma_f32_16x16x32_bf16 v[140:143], a[84:87], a[60:63], v[140:143]\n\t"
    "ds_read_b64_tr_b16 a[70:71], v242 offset:7328\n\t"
    "v_mfma_f32_16x16x32_bf16 v[140:143], a[80:83], a[44:47], v[140:143]\n\t"
    "buffer_load_dwordx4 v235, s[40:43], s59 offen offset:3072 lds\n\t"
    "s_waitcnt lgkmcnt(6)\n\t"
    "v_mfma_f32_16x16x32_bf16 v[156:159], a[88:91], a[44:47], v[156:159]\n\t"
    "ds_read_b64_tr_b16 a[72:73], v241 offset:14496\n\t"
    "v_mfma_f32_16x16x32_bf16 v[156:159], a[92:95], a[60:63], v[156:159]\n\t"
    "v_mfma_f32_16x16x32_bf16 v[152:155], a[92:95], a[56:59], v[152:155]\n\t"
    "ds_read_b64_tr_b16 a[74:75], v242 offset:14496\n\t"
    "v_mfma_f32_16x16x32_bf16 v[152:155], a[88:91], a[40:43], v[152:155]\n\t"
    "ds_read_b128 a[12:15], v243 offset:6144\n\t"
    "v_mfma_f32_16x16x32_bf16 v[148:151], a[88:91], a[36:39], v[148:151]\n\t"
    "ds_read_b64_tr_b16 a[76:77], v241 offset:21664\n\t"
    "v_mfma_f32_16x16x32_bf16 v[148:151], a[92:95], a[52:55], v[148:151]\n\t"
    "ds_read_b128 a[28:31], v244 offset:6144\n\t"
    "v_mfma_f32_16x16x32_bf16 v[144:147], a[92:95], a[48:51], v[144:147]\n\t"
    "ds_read_b64_tr_b16 a[78:79], v242 offset:21664\n\t"
    "s_add_u32 m0, s61, 4096\n\t"
    "v_mfma_f32_16x16x32_bf16 v[144:147], a[88:91], a[32:35], v[144:147]\n\t"
    "buffer_load_dwordx4 v236, s[40:43], s59 offen offset:0 lds\n\t"
    "s_waitcnt lgkmcnt(6)\n\t"
    "v_mfma_f32_16x16x32_bf16 v[160:163], a[64:67], a[32:35], v[160:163]\n\t"
    "ds_read_b64_tr_b16 a[80:81], v241 offset:192\n\t"
    "v_mfma_f32_16x16x32_bf16 v[160:163], a[68:71], a[48:51], v[160:163]\n\t"
    "ds_read_b64_tr_b16 a[82:83], v242 offset:192\n\t"
    "v_mfma_f32_16x16x32_bf16 v[164:167], a[68:71], a[52:55], v[164:167]\n\t"
    "ds_read_b64_tr_b16 a[84:85], v241 offset:7360\n\t"
    "v_mfma_f32_16x16x32_bf16 v[164:167], a[64:67], a[36:39], v[164:167]\n\t"
    "ds_read_b64_tr_b16 a[86:87], v242 offset:7360\n\t"
    "v_mfma_f32_16x16x32_bf16 v[168:171], a[64:67], a[40:43], v[168:171]\n\t"
    "ds_read_b64_tr_b16 a[88:89], v241 offset:14528\n\t"
    "v_mfma_f32_16x16x32_bf16 v[168:171], a[68:71], a[56:59], v[168:171]\n\t"
    "ds_read_b64_tr_b16 a[90:91], v242 offset:14528\n\t"
    "buffer_load_dwordx4 v237, s[40:43], s59 offen offset:1024 lds\n\t"
    "v_mfma_f32_16x16x32_bf16 v[172:175], a[68:71], a[60:63], v[172:175]\n\t"
    "ds_read_b64_tr_b16 a[92:93], v241 offset:21696\n\t"
    "v_mfma_f32_16x16x32_bf16 v[172:175], a[64:67], a[44:47], v[172:175]\n\t"
    "ds_read_b64_tr_b16 a[94:95], v242 offset:21696\n\t"
    "s_waitcnt lgkmcnt(8)\n\t"
    "v_mfma_f32_16x16x32_bf16 v[188:191], a[72:75], a[44:47], v[188:191]\n\t"
    "v_mfma_f32_16x16x32_bf16 v[188:191], a[76:79], a[60:63], v[188:191]\n\t"
    "v_mfma_f32_16x16x32_bf16 v[184:187], a[76:79], a[56:59], v[184:187]\n\t"
    "v_mfma_f32_16x16x32_bf16 v[184:187], a[72:75], a[40:43], v[184:187]\n\t"
    "buffer_load_dwordx4 v238, s[40:43], s59 offen offset:2048 lds\n\t"
    "s_mov_b32 s57, s56\n\t"
    "v_mfma_f32_16x16x32_bf16 v[180:183], a[72:75], a[36:39], v[180:183]\n\t"
    "s_add_u32 s56, s56, 32768\n\t"
    "v_mfma_f32_16x16x32_bf16 v[180:183], a[76:79], a[52:55], v[180:183]\n\t"
    "s_cmp_eq_u32 s56, 98304\n\t"
    "v_mfma_f32_16x16x32_bf16 v[176:179], a[76:79], a[48:51], v[176:179]\n\t"
    "s_cselect_b32 s56, 0, s56\n\t"
    "v_mfma_f32_16x16x32_bf16 v[176:179], a[72:75], a[32:35], v[176:179]\n\t"
    "s_and_b32 s61, s52, 15\n\t"
    "s_waitcnt lgkmcnt(4)\n\t"
    "v_mfma_f32_16x16x32_bf16 v[192:195], a[80:83], a[32:35], v[192:195]\n\t"
    "s_lshr_b64 s[52:53], s[52:53], 4\n\t"
    "v_mfma_f32_16x16x32_bf16 v[192:195], a[84:87], a[48:51], v[192:195]\n\t"
    "s_lshl_b32 s58, s61, 7\n\t"
    "v_mfma_f32_16x16x32_bf16 v[196:199], a[84:87], a[52:55], v[196:199]\n\t"
    "s_cmp_eq_u32 s60, s69\n\t"
    "s_mul_i32 s59, s61, 458752\n\t"
    "v_mfma_f32_16x16x32_bf16 v[196:199], a[80:83], a[36:39], v[196:199]\n\t"
    "s_cbranch_scc1 PFBC_%=\n\t"
    "PFRC_%=:\n\t"
    "s_sub_u32 s60, s60, 1\n\t"
    "v_mfma_f32_16x16x32_bf16 v[200:203], a[80:83], a[40:43], v[200:203]\n\t"
    "v_mfma_f32_16x16x32_bf16 v[200:203], a[84:87], a[56:59], v[200:203]\n\t"
    "v_add_u32 v241, s56, v239\n\t"
    "v_mfma_f32_16x16x32_bf16 v[204:207], a[84:87], a[60:63], v[204:207]\n\t"
    "v_add_u32 v242, s56, v240\n\t"
    "v_mfma_f32_16x16x32_bf16 v[204:207], a[80:83], a[44:47], v[204:207]\n\t"
    "s_waitcnt vmcnt(15) lgkmcnt(0)\n\t"
    "s_barrier\n\t"
    "v_mfma_f32_16x16x32_bf16 v[220:223], a[88:91], a[44:47], v[220:223]\n\t"
    "ds_read_b64_tr_b16 a[64:65], v241 offset:0\n\t"
    "v_mfma_f32_16x16x32_bf16 v[220:223], a[92:95], a[60:63], v[220:223]\n\t"
    "ds_read_b64_tr_b16 a[66:67], v242 offset:0\n\t"
    "s_cmp_eq_u32 s60, 2\n\t"
    "v_mfma_f32_16x16x32_bf16 v[216:219], a[92:95], a[56:59], v[216:219]\n\t"
    "ds_read_b64_tr_b16 a[68:69], v241 offset:7168\n\t"
    "s_cbranch_scc1 G1FIX_%=\n\t"
    "G1BACK_%=:\n\t"
    "v_mfma_f32_16x16x32_bf16 v[216:219], a[88:91], a[40:43], v[216:219]\n\t"
    "ds_read_b64_tr_b16 a[70:71], v242 offset:7168\n\t"
    "v_mfma_f32_16x16x32_bf16 v[212:215], a[88:91], a[36:39], v[212:215]\n\t"
    "ds_read_b64_tr_b16 a[72:73], v241 offset:14336\n\t"
    "v_mfma_f32_16x16x32_bf16 v[212:215], a[92:95], a[52:55], v[212:215]\n\t"
    "ds_read_b64_tr_b16 a[74:75], v242 offset:14336\n\t"
    "v_mfma_f32_16x16x32_bf16 v[208:211], a[92:95], a[48:51], v[208:211]\n\t"
    "ds_read_b64_tr_b16 a[76:77], v241 offset:21504\n\t"
    "s_cmp_lg_u32 s60, 0\n\t"
    "v_mfma_f32_16x16x32_bf16 v[208:211], a[88:91], a[32:35], v[208:211]\n\t"
    "ds_read_b64_tr_b16 a[78:79], v242 offset:21504\n\t"
    "s_cbranch_scc1 G1LOOP_%=\n\t"
    "TAIL_%=:\n\t"
    "s_waitcnt lgkmcnt(4)\n\t"
    "v_mfma_f32_16x16x32_bf16 v[0:3], a[64:67], a[0:3], v[0:3]\n\t"
    "ds_read_b64_tr_b16 a[80:81], v241 offset:32\n\t"
    "v_mfma_f32_16x16x32_bf16 v[0:3], a[68:71], a[16:19], v[0:3]\n\t"
    "ds_read_b64_tr_b16 a[82:83], v242 offset:32\n\t"
    "v_mfma_f32_16x16x32_bf16 v[4:7], a[68:71], a[20:23], v[4:7]\n\t"
    "ds_read_b64_tr_b16 a[84:85], v241 offset:7200\n\t"
    "v_accvgpr_read_b32 v224, a224\n\t"
    "v_mfma_f32_16x16x32_bf16 v[4:7], a[64:67], a[4:7], v[4:7]\n\t"
    "ds_read_b64_tr_b16 a[86:87], v242 offset:7200\n\t"
    "v_xor_b32 v246, 64, v224\n\t"
    "v_mfma_f32_16x16x32_bf16 v[8:11], a[64:67], a[8:11], v[8:11]\n\t"
    "v_add_u32 v225, 15360, v246\n\t"
    "v_mfma_f32_16x16x32_bf16 v[8:11], a[68:71], a[24:27], v[8:11]\n\t"
    "v_add_u32 v226, 30720, v224\n\t"
    "v_mfma_f32_16x16x32_bf16 v[12:15], a[68:71], a[28:31], v[12:15]\n\t"
    "v_add_u32 v227, 46080, v246\n\t"
    "v_mfma_f32_16x16x32_bf16 v[12:15], a[64:67], a[12:15], v[12:15]\n\t"
    "v_add_u32 v228, 65536, v224\n\t"
    "s_waitcnt lgkmcnt(4)\n\t"
    "v_mfma_f32_16x16x32_bf16 v[28:31], a[72:75], a[12:15], v[28:31]\n\t"
    "ds_read_b64_tr_b16 a[88:89], v241 offset:14368\n\t"
    "v_add_u32 v229, 80896, v246\n\t"
    "v_mfma_f32_16x16x32_bf16 v[28:31], a[76:79], a[28:31], v[28:31]\n\t"
    "ds_read_b64_tr_b16 a[90:91], v242 offset:14368\n\t"
    "v_add_u32 v230, 96256, v224\n\t"
    "v_mfma_f32_16x16x32_bf16 v[24:27], a[76:79], a[24:27], v[24:27]\n\t"
    "ds_read_b64_tr_b16 a[92:93], v241 offset:21536\n\t"
    "v_add_u32 v231, 111616, v246\n\t"
    "v_mfma_f32_16x16x32_bf16 v[24:27], a[72:75], a[8:11], v[24:27]\n\t"
    "ds_read_b64_tr_b16 a[94:95], v242 offset:21536\n\t"
    "s_waitcnt vmcnt(7)\n\t"
    "v_mfma_f32_16x16x32_bf16 v[20:23], a[72:75], a[4:7], v[20:23]\n\t"
    "ds_read_b128 a[32:35], v243 offset:32768\n\t"
    "v_mfma_f32_16x16x32_bf16 v[20:23], a[76:79], a[20:23], v[20:23]\n\t"
    "ds_read_b128 a[48:51], v244 offset:32768\n\t"
    "v_mfma_f32_16x16x32_bf16 v[16:19], a[76:79], a[16:19], v[16:19]\n\t"
    "ds_read_b128 a[36:39], v243 offset:34816\n\t"
    "v_mfma_f32_16x16x32_bf16 v[16:19], a[72:75], a[0:3], v[16:19]\n\t"
    "ds_read_b128 a[52:55], v244 offset:34816\n\t"
    "s_waitcnt lgkmcnt(8)\n\t"
    "v_mfma_f32_16x16x32_bf16 v[32:35], a[80:83], a[0:3], v[32:35]\n\t"
    "ds_read_b128 a[40:43], v243 offset:36864\n\t"
    "v_mfma_f32_16x16x32_bf16 v[32:35], a[84:87], a[16:19], v[32:35]\n\t"
    "ds_read_b128 a[56:59], v244 offset:36864\n\t"
    "v_mfma_f32_16x16x32_bf16 v[36:39], a[84:87], a[20:23], v[36:39]\n\t"
    "ds_read_b128 a[44:47], v243 offset:38912\n\t"
    "v_mfma_f32_16x16x32_bf16 v[36:39], a[80:83], a[4:7], v[36:39]\n\t"
    "ds_read_b128 a[60:63], v244 offset:38912\n\t"
    "v_mfma_f32_16x16x32_bf16 v[40:43], a[80:83], a[8:11], v[40:43]\n\t"
    "s_add_u32 s58, s70, 0\n\t"
    "s_add_u32 m0, s72, 98304\n\t"
    "v_mfma_f32_16x16x32_bf16 v[40:43], a[84:87], a[24:27], v[40:43]\n\t"
    "buffer_load_dwordx4 v224, s[48:51], s58 offen offset:0 lds\n\t"
    "s_waitcnt vmcnt(1) lgkmcnt(5)\n\t"
    "s_barrier\n\t"
    "v_mfma_f32_16x16x32_bf16 v[44:47], a[84:87], a[28:31], v[44:47]\n\t"
    "ds_read_b64_tr_b16 a[64:65], v239 offset:0\n\t"
    "ds_read_b64_tr_b16 a[66:67], v240 offset:0\n\t"
    "v_mfma_f32_16x16x32_bf16 v[44:47], a[80:83], a[12:15], v[44:47]\n\t"
    "ds_read_b64_tr_b16 a[68:69], v239 offset:7168\n\t"
    "ds_read_b64_tr_b16 a[70:71], v240 offset:7168\n\t"
    "v_mfma_f32_16x16x32_bf16 v[60:63], a[88:91], a[12:15], v[60:63]\n\t"
    "buffer_load_dwordx4 v225, s[48:51], s58 offen offset:1024 lds\n\t"
    "v_mfma_f32_16x16x32_bf16 v[60:63], a[92:95], a[28:31], v[60:63]\n\t"
    "v_mfma_f32_16x16x32_bf16 v[56:59], a[92:95], a[24:27], v[56:59]\n\t"
    "ds_read_b64_tr_b16 a[72:73], v239 offset:14336\n\t"
    "v_mfma_f32_16x16x32_bf16 v[56:59], a[88:91], a[8:11], v[56:59]\n\t"
    "buffer_load_dwordx4 v226, s[48:51], s58 offen offset:2048 lds\n\t"
    "v_mfma_f32_16x16x32_bf16 v[52:55], a[88:91], a[4:7], v[52:55]\n\t"
    "ds_read_b64_tr_b16 a[74:75], v240 offset:14336\n\t"
    "v_mfma_f32_16x16x32_bf16 v[52:55], a[92:95], a[20:23], v[52:55]\n\t"
    "v_mfma_f32_16x16x32_bf16 v[48:51], a[92:95], a[16:19], v[48:51]\n\t"
    "ds_read_b64_tr_b16 a[76:77], v239 offset:21504\n\t"
    "buffer_load_dwordx4 v227, s[48:51], s58 offen offset:3072 lds\n\t"
    "v_mfma_f32_16x16x32_bf16 v[48:51], a[88:91], a[0:3], v[48:51]\n\t"
    "s_waitcnt lgkmcnt(3)\n\t"
    "v_mfma_f32_16x16x32_bf16 v[0:3], a[64:67], a[32:35], v[0:3]\n\t"
    "ds_read_b64_tr_b16 a[78:79], v240 offset:21504\n\t"
    "ds_read_b64_tr_b16 a[80:81], v239 offset:32\n\t"
    "s_add_u32 s58, s70, 0\n\t"
    "s_add_u32 m0, s72, 102400\n\t"
    "v_mfma_f32_16x16x32_bf16 v[0:3], a[68:71], a[48:51], v[0:3]\n\t"
    "buffer_load_dwordx4 v228, s[48:51], s58 offen offset:0 lds\n\t"
    "v_mfma_f32_16x16x32_bf16 v[4:7], a[68:71], a[52:55], v[4:7]\n\t"
    "ds_read_b64_tr_b16 a[82:83], v240 offset:32\n\t"
    "v_mfma_f32_16x16x32_bf16 v[4:7], a[64:67], a[36:39], v[4:7]\n\t"
    "v_mfma_f32_16x16x32_bf16 v[8:11], a[64:67], a[40:43], v[8:11]\n\t"
    "ds_read_b64_tr_b16 a[84:85], v239 offset:7200\n\t"
    "buffer_load_dwordx4 v229, s[48:51], s58 offen offset:1024 lds\n\t"
    "s_lshl_b32 s36, s33, 10\n\t"
    "v_mfma_f32_16x16x32_bf16 v[8:11], a[68:71], a[56:59], v[8:11]\n\t"
    "s_add_u32 s37, s36, 229632\n\t"
    "v_mfma_f32_16x16x32_bf16 v[12:15], a[68:71], a[60:63], v[12:15]\n\t"
    "ds_read_b64_tr_b16 a[86:87], v240 offset:7200\n\t"
    "s_lshl_b32 s38, s33, 11\n\t"
    "v_mfma_f32_16x16x32_bf16 v[12:15], a[64:67], a[44:47], v[12:15]\n\t"
    "buffer_load_dwordx4 v230, s[48:51], s58 offen offset:2048 lds\n\t"
    "s_add_u32 s38, s38, 32768\n\t"
    "s_waitcnt lgkmcnt(4)\n\t"
    "v_mfma_f32_16x16x32_bf16 v[28:31], a[72:75], a[44:47], v[28:31]\n\t"
    "ds_read_b64_tr_b16 a[88:89], v239 offset:14368\n\t"
    "s_add_u32 s39, s38, 229632\n\t"
    "v_mfma_f32_16x16x32_bf16 v[28:31], a[76:79], a[60:63], v[28:31]\n\t"
    "s_lshl_b32 s40, s33, 9\n\t"
    "v_mfma_f32_16x16x32_bf16 v[24:27], a[76:79], a[56:59], v[24:27]\n\t"
    "ds_read_b64_tr_b16 a[90:91], v240 offset:14368\n\t"
    "buffer_load_dwordx4 v231, s[48:51], s58 offen offset:3072 lds\n\t"
    "s_add_u32 s40, s40, 98304\n\t"
    "v_mfma_f32_16x16x32_bf16 v[24:27], a[72:75], a[40:43], v[24:27]\n\t"
    "s_add_u32 s41, s40, 229632\n\t"
    "v_mfma_f32_16x16x32_bf16 v[20:23], a[72:75], a[36:39], v[20:23]\n\t"
    "ds_read_b64_tr_b16 a[92:93], v239 offset:21536\n\t"
    "s_add_u32 s58, s30, 0\n\t"
    "s_add_u32 m0, s72, 0\n\t"
    "v_mov_b32 v232, s74\n\t"
    "v_mfma_f32_16x16x32_bf16 v[20:23], a[76:79], a[52:55], v[20:23]\n\t"
    "buffer_load_dwordx4 v224, s[48:51], s58 offen offset:0 lds\n\t"
    "v_mov_b32 v233, s75\n\t"
    "v_mfma_f32_16x16x32_bf16 v[16:19], a[76:79], a[48:51], v[16:19]\n\t"
    "ds_read_b64_tr_b16 a[94:95], v240 offset:21536\n\t"
    "v_accvgpr_read_b32 v236, a225\n\t"
    "v_mfma_f32_16x16x32_bf16 v[16:19], a[72:75], a[32:35], v[16:19]\n\t"
    "v_accvgpr_read_b32 v237, a226\n\t"
    "s_waitcnt lgkmcnt(4)\n\t"
    "v_mfma_f32_16x16x32_bf16 v[32:35], a[80:83], a[32:35], v[32:35]\n\t"
    "ds_read_b64_tr_b16 a[64:65], v241 offset:64\n\t"
    "buffer_load_dwordx4 v225, s[48:51], s58 offen offset:1024 lds\n\t"
    "v_mfma_f32_16x16x32_bf16 v[32:35], a[84:87], a[48:51], v[32:35]\n\t"
    "v_mfma_f32_16x16x32_bf16 v[36:39], a[84:87], a[52:55], v[36:39]\n\t"
    "ds_read_b64_tr_b16 a[66:67], v242 offset:64\n\t"
    "v_mfma_f32_16x16x32_bf16 v[36:39], a[80:83], a[36:39], v[36:39]\n\t"
    "buffer_load_dwordx4 v226, s[48:51], s58 offen offset:2048 lds\n\t"
    "v_mfma_f32_16x16x32_bf16 v[40:43], a[80:83], a[40:43], v[40:43]\n\t"
    "ds_read_b64_tr_b16 a[68:69], v241 offset:7232\n\t"
    "v_mfma_f32_16x16x32_bf16 v[40:43], a[84:87], a[56:59], v[40:43]\n\t"
    "v_mfma_f32_16x16x32_bf16 v[44:47], a[84:87], a[60:63], v[44:47]\n\t"
    "ds_read_b64_tr_b16 a[70:71], v242 offset:7232\n\t"
    "buffer_load_dwordx4 v227, s[48:51], s58 offen offset:3072 lds\n\t"
    "v_mul_f32 v246, s62, v0\n\t"
    "v_mul_f32 v247, s62, v1\n\t"
    "v_mul_f32 v248, s62, v2\n\t"
    "v_mul_f32 v249, s62, v3\n\t"
    "v_mul_f32 v250, s62, v4\n\t"
    "v_mul_f32 v251, s62, v5\n\t"
    "v_mfma_f32_16x16x32_bf16 v[44:47], a[80:83], a[44:47], v[44:47]\n\t"
    "v_mul_f32 v252, s62, v6\n\t"
    "v_mul_f32 v253, s62, v7\n\t"
    "v_exp_f32 v246, v246\n\t"
    "v_exp_f32 v247, v247\n\t"
    "s_waitcnt lgkmcnt(4)\n\t"
    "v_mfma_f32_16x16x32_bf16 v[60:63], a[88:91], a[44:47], v[60:63]\n\t"
    "ds_read_b64_tr_b16 a[72:73], v239 offset:64\n\t"
    "s_add_u32 s58, s30, 163840\n\t"
    "s_add_u32 m0, s72, 4096\n\t"
    "v_exp_f32 v248, v248\n\t"
    "v_exp_f32 v249, v249\n\t"
    "v_exp_f32 v250, v250\n\t"
    "v_mfma_f32_16x16x32_bf16 v[60:63], a[92:95], a[60:63], v[60:63]\n\t"
    "buffer_load_dwordx4 v228, s[48:51], s58 offen offset:0 lds\n\t"
    "v_exp_f32 v251, v251\n\t"
    "v_exp_f32 v252, v252\n\t"
    "v_mfma_f32_16x16x32_bf16 v[56:59], a[92:95], a[56:59], v[56:59]\n\t"
    "ds_read_b64_tr_b16 a[74:75], v240 offset:64\n\t"
    "v_exp_f32 v253, v253\n\t"
    "v_add_f32 v246, 1.0, v246\n\t"
    "v_add_f32 v247, 1.0, v247\n\t"
    "v_add_f32 v248, 1.0, v248\n\t"
    "v_add_f32 v249, 1.0, v249\n\t"
    "v_mfma_f32_16x16x32_bf16 v[56:59], a[88:91], a[40:43], v[56:59]\n\t"
    "v_add_f32 v250, 1.0, v250\n\t"
    "v_add_f32 v251, 1.0, v251\n\t"
    "v_add_f32 v252, 1.0, v252\n\t"
    "v_add_f32 v253, 1.0, v253\n\t"
    "v_rcp_f32 v246, v246\n\t"
    "v_mfma_f32_16x16x32_bf16 v[52:55], a[88:91], a[36:39], v[52:55]\n\t"
    "ds_read_b64_tr_b16 a[76:77], v239 offset:7232\n\t"
    "buffer_load_dwordx4 v229, s[48:51], s58 offen offset:1024 lds\n\t"
    "v_rcp_f32 v247, v247\n\t"
    "v_rcp_f32 v248, v248\n\t"
    "v_rcp_f32 v249, v249\n\t"
    "v_mfma_f32_16x16x32_bf16 v[52:55], a[92:95], a[52:55], v[52:55]\n\t"
    "v_rcp_f32 v250, v250\n\t"
    "v_rcp_f32 v251, v251\n\t"
    "v_mfma_f32_16x16x32_bf16 v[48:51], a[92:95], a[48:51], v[48:51]\n\t"
    "ds_read_b64_tr_b16 a[78:79], v240 offset:7232\n\t"
    "v_rcp_f32 v252, v252\n\t"
    "v_rcp_f32 v253, v253\n\t"
    "v_mul_f32 v0, v246, v0\n\t"
    "v_mul_f32 v1, v247, v1\n\t"
    "v_mfma_f32_16x16x32_bf16 v[48:51], a[88:91], a[32:35], v[48:51]\n\t"
    "buffer_load_dwordx4 v230, s[48:51], s58 offen offset:2048 lds\n\t"
    "v_mul_f32 v2, v248, v2\n\t"
    "v_mul_f32 v3, v249, v3\n\t"
    "v_mul_f32 v4, v250, v4\n\t"
    "v_mul_f32 v5, v251, v5\n\t"
    "v_mul_f32 v6, v252, v6\n\t"
    "s_waitcnt lgkmcnt(4)\n\t"
    "v_mfma_f32_16x16x32_bf16 v[64:67], a[64:67], a[0:3], v[64:67]\n\t"
    "ds_read_b64_tr_b16 a[80:81], v241 offset:14400\n\t"
    "v_mul_f32 v7, v253, v7\n\t"
    "v_mul_f32 v0, v16, v0\n\t"
    "v_mul_f32 v1, v17, v1\n\t"
    "v_mul_f32 v2, v18, v2\n\t"
    "v_mul_f32 v3, v19, v3\n\t"
    "v_mul_f32 v4, v20, v4\n\t"
    "v_mfma_f32_16x16x32_bf16 v[64:67], a[68:71], a[16:19], v[64:67]\n\t"
    "v_mul_f32 v5, v21, v5\n\t"
    "v_mul_f32 v6, v22, v6\n\t"
    "v_mul_f32 v7, v23, v7\n\t"
    "v_cvt_pk_bf16_f32 v16, v0, v1\n\t"
    "v_cvt_pk_bf16_f32 v18, v4, v5\n\t"
    "v_mfma_f32_16x16x32_bf16 v[68:71], a[68:71], a[20:23], v[68:71]\n\t"
    "ds_read_b64_tr_b16 a[82:83], v242 offset:14400\n\t"
    "buffer_load_dwordx4 v231, s[48:51], s58 offen offset:3072 lds\n\t"
    "v_cvt_pk_bf16_f32 v17, v2, v3\n\t"
    "v_cvt_pk_bf16_f32 v19, v6, v7\n\t"
    "v_permlane16_swap_b32 v16, v18\n\t"
    "s_nop 0\n\t"
    "v_permlane16_swap_b32 v17, v19\n\t"
    "buffer_store_dwordx4 v[16:19], v245, s[44:47], s36 offen offset:0 sc1\n\t"
    "v_mfma_f32_16x16x32_bf16 v[68:71], a[64:67], a[4:7], v[68:71]\n\t"
    "v_mul_f32 v246, s62, v8\n\t"
    "v_mul_f32 v247, s62, v9\n\t"
    "v_mul_f32 v248, s62, v10\n\t"
    "v_mul_f32 v249, s62, v11\n\t"
    "v_mul_f32 v250, s62, v12\n\t"
    "v_mfma_f32_16x16x32_bf16 v[72:75], a[64:67], a[8:11], v[72:75]\n\t"
    "ds_read_b64_tr_b16 a[84:85], v241 offset:21568\n\t"
    "s_add_u32 s58, s30, 917504\n\t"
    "s_add_u32 m0, s72, 32768\n\t"
    "v_mul_f32 v251, s62, v13\n\t"
    "v_mul_f32 v252, s62, v14\n\t"
    "v_mul_f32 v253, s62, v15\n\t"
    "v_exp_f32 v246, v246\n\t"
    "v_exp_f32 v247, v247\n\t"
    "v_mfma_f32_16x16x32_bf16 v[72:75], a[68:71], a[24:27], v[72:75]\n\t"
    "buffer_load_dwordx4 v224, s[48:51], s58 offen offset:0 lds\n\t"
    "v_exp_f32 v248, v248\n\t"
    "v_exp_f32 v249, v249\n\t"
    "v_mfma_f32_16x16x32_bf16 v[76:79], a[68:71], a[28:31], v[76:79]\n\t"
    "ds_read_b64_tr_b16 a[86:87], v242 offset:21568\n\t"
    "v_exp_f32 v250, v250\n\t"
    "v_exp_f32 v251, v251\n\t"
    "v_exp_f32 v252, v252\n\t"
    "v_mfma_f32_16x16x32_bf16 v[76:79], a[64:67], a[12:15], v[76:79]\n\t"
    "v_exp_f32 v253, v253\n\t"
    "v_add_f32 v246, 1.0, v246\n\t"
    "v_add_f32 v247, 1.0, v247\n\t"
    "v_add_f32 v248, 1.0, v248\n\t"
    "s_waitcnt lgkmcnt(4)\n\t"
    "v_mfma_f32_16x16x32_bf16 v[76:79], a[72:75], a[44:47], v[76:79]\n\t"
    "ds_read_b64_tr_b16 a[88:89], v239 offset:14400\n\t"
    "buffer_load_dwordx4 v225, s[48:51], s58 offen offset:1024 lds\n\t"
    "v_add_f32 v249, 1.0, v249\n\t"
    "v_add_f32 v250, 1.0, v250\n\t"
    "v_add_f32 v251, 1.0, v251\n\t"
    "v_add_f32 v252, 1.0, v252\n\t"
    "v_add_f32 v253, 1.0, v253\n\t"
    "v_rcp_f32 v246, v246\n\t"
    "v_mfma_f32_16x16x32_bf16 v[76:79], a[76:79], a[60:63], v[76:79]\n\t"
    "v_rcp_f32 v247, v247\n\t"
    "v_rcp_f32 v248, v248\n\t"
    "v_mfma_f32_16x16x32_bf16 v[72:75], a[76:79], a[56:59], v[72:75]\n\t"
    "ds_read_b64_tr_b16 a[90:91], v240 offset:14400\n\t"
    "v_rcp_f32 v249, v249\n\t"
    "v_rcp_f32 v250, v250\n\t"
    "v_rcp_f32 v251, v251\n\t"
    "v_mfma_f32_16x16x32_bf16 v[72:75], a[72:75], a[40:43], v[72:75]\n\t"
    "buffer_load_dwordx4 v226, s[48:51], s58 offen offset:2048 lds\n\t"
    "v_rcp_f32 v252, v252\n\t"
    "v_rcp_f32 v253, v253\n\t"
    "v_mul_f32 v8, v246, v8\n\t"
    "v_mfma_f32_16x16x32_bf16 v[68:71], a[72:75], a[36:39], v[68:71]\n\t"
    "ds_read_b64_tr_b16 a[92:93], v239 offset:21568\n\t"
    "v_mul_f32 v9, v247, v9\n\t"
    "v_mul_f32 v10, v248, v10\n\t"
    "v_mul_f32 v11, v249, v11\n\t"
    "v_mul_f32 v12, v250, v12\n\t"
    "v_mul_f32 v13, v251, v13\n\t"
    "v_mul_f32 v14, v252, v14\n\t"
    "v_mfma_f32_16x16x32_bf16 v[68:71], a[76:79], a[52:55], v[68:71]\n\t"
    "v_mul_f32 v15, v253, v15\n\t"
    "v_mul_f32 v8, v24, v8\n\t"
    "v_mul_f32 v9, v25, v9\n\t"
    "v_mul_f32 v10, v26, v10\n\t"
    "v_mul_f32 v11, v27, v11\n\t"
    "v_mfma_f32_16x16x32_bf16 v[64:67], a[76:79], a[48:51], v[64:67]\n\t"
    "ds_read_b64_tr_b16 a[94:95], v240 offset:21568\n\t"
    "buffer_load_dwordx4 v227, s[48:51], s58 offen offset:3072 lds\n\t"
    "v_mul_f32 v12, v28, v12\n\t"
    "v_mul_f32 v13, v29, v13\n\t"
    "v_mul_f32 v14, v30, v14\n\t"
    "v_mul_f32 v15, v31, v15\n\t"
    "v_cvt_pk_bf16_f32 v24, v8, v9\n\t"
    "v_cvt_pk_bf16_f32 v26, v12, v13\n\t"
    "v_mfma_f32_16x16x32_bf16 v[64:67], a[72:75], a[32:35], v[64:67]\n\t"
    "v_cvt_pk_bf16_f32 v25, v10, v11\n\t"
    "v_cvt_pk_bf16_f32 v27, v14, v15\n\t"
    "v_permlane16_swap_b32 v24, v26\n\t"
    "s_nop 0\n\t"
    "v_permlane16_swap_b32 v25, v27\n\t"
    "s_waitcnt lgkmcnt(4)\n\t"
    "v_mfma_f32_16x16x32_bf16 v[80:83], a[80:83], a[0:3], v[80:83]\n\t"
    "ds_read_b64_tr_b16 a[64:65], v241 offset:96\n\t"
    "s_add_u32 s58, s30, 1081344\n\t"
    "s_add_u32 m0, s72, 36864\n\t"
    "buffer_store_dwordx4 v[24:27], v245, s[44:47], s37 offen offset:0 sc1\n\t"
    "v_mul_f32 v246, s62, v32\n\t"
    "v_mul_f32 v247, s62, v33\n\t"
    "v_mul_f32 v248, s62, v34\n\t"
    "v_mul_f32 v249, s62, v35\n\t"
    "v_mul_f32 v250, s62, v36\n\t"
    "v_mfma_f32_16x16x32_bf16 v[80:83], a[84:87], a[16:19], v[80:83]\n\t"
    "buffer_load_dwordx4 v228, s[48:51], s58 offen offset:0 lds\n\t"
    "v_mul_f32 v251, s62, v37\n\t"
    "v_mul_f32 v252, s62, v38\n\t"
    "v_mul_f32 v253, s62, v39\n\t"
    "v_exp_f32 v246, v246\n\t"
    "v_mfma_f32_16x16x32_bf16 v[84:87], a[84:87], a[20:23], v[84:87]\n\t"
    "ds_read_b64_tr_b16 a[66:67], v242 offset:96\n\t"
    "v_exp_f32 v247, v247\n\t"
    "v_exp_f32 v248, v248\n\t"
    "v_exp_f32 v249, v249\n\t"
    "v_mfma_f32_16x16x32_bf16 v[84:87], a[80:83], a[4:7], v[84:87]\n\t"
    "v_exp_f32 v250, v250\n\t"
    "v_exp_f32 v251, v251\n\t"
    "v_exp_f32 v252, v252\n\t"
    "v_mfma_f32_16x16x32_bf16 v[88:91], a[80:83], a[8:11], v[88:91]\n\t"
    "ds_read_b64_tr_b16 a[68:69], v241 offset:7264\n\t"
    "buffer_load_dwordx4 v229, s[48:51], s58 offen offset:1024 lds\n\t"
    "v_exp_f32 v253, v253\n\t"
    "v_add_f32 v246, 1.0, v246\n\t"
    "v_add_f32 v247, 1.0, v247\n\t"
    "v_add_f32 v248, 1.0, v248\n\t"
    "v_mfma_f32_16x16x32_bf16 v[88:91], a[84:87], a[24:27], v[88:91]\n\t"
    "v_add_f32 v249, 1.0, v249\n\t"
    "v_add_f32 v250, 1.0, v250\n\t"
    "v_add_f32 v251, 1.0, v251\n\t"
    "v_add_f32 v252, 1.0, v252\n\t"
    "v_add_f32 v253, 1.0, v253\n\t"
    "v_mfma_f32_16x16x32_bf16 v[92:95], a[84:87], a[28:31], v[92:95]\n\t"
    "ds_read_b64_tr_b16 a[70:71], v242 offset:7264\n\t"
    "v_rcp_f32 v246, v246\n\t"
    "v_rcp_f32 v247, v247\n\t"
    "v_rcp_f32 v248, v248\n\t"
    "v_mfma_f32_16x16x32_bf16 v[92:95], a[80:83], a[12:15], v[92:95]\n\t"
    "buffer_load_dwordx4 v230, s[48:51], s58 offen offset:2048 lds\n\t"
    "v_rcp_f32 v249, v249\n\t"
    "v_rcp_f32 v250, v250\n\t"
    "v_rcp_f32 v251, v251\n\t"
    "s_waitcnt lgkmcnt(4)\n\t"
    "v_mfma_f32_16x16x32_bf16 v[92:95], a[88:91], a[44:47], v[92:95]\n\t"
    "ds_read_b64_tr_b16 a[72:73], v239 offset:96\n\t"
    "v_rcp_f32 v252, v252\n\t"
    "v_rcp_f32 v253, v253\n\t"
    "v_mul_f32 v32, v246, v32\n\t"
    "v_mfma_f32_16x16x32_bf16 v[92:95], a[92:95], a[60:63], v[92:95]\n\t"
    "v_mul_f32 v33, v247, v33\n\t"
    "v_mul_f32 v34, v248, v34\n\t"
    "v_mul_f32 v35, v249, v35\n\t"
    "v_mul_f32 v36, v250, v36\n\t"
    "v_mul_f32 v37, v251, v37\n\t"
    "v_mfma_f32_16x16x32_bf16 v[88:91], a[92:95], a[56:59], v[88:91]\n\t"
    "ds_read_b64_tr_b16 a[74:75], v240 offset:96\n\t"
    "buffer_load_dwordx4 v231, s[48:51], s58 offen offset:3072 lds\n\t"
    "v_mul_f32 v38, v252, v38\n\t"
    "v_mul_f32 v39, v253, v39\n\t"
    "v_mul_f32 v32, v48, v32\n\t"
    "v_mul_f32 v33, v49, v33\n\t"
    "v_mul_f32 v34, v50, v34\n\t"
    "v_mul_f32 v35, v51, v35\n\t"
    "v_mfma_f32_16x16x32_bf16 v[88:91], a[88:91], a[40:43], v[88:91]\n\t"
    "v_mul_f32 v36, v52, v36\n\t"
    "v_mul_f32 v37, v53, v37\n\t"
    "v_mul_f32 v38, v54, v38\n\t"
    "v_mul_f32 v39, v55, v39\n\t"
    "v_cvt_pk_bf16_f32 v48, v32, v33\n\t"
    "v_mfma_f32_16x16x32_bf16 v[84:87], a[88:91], a[36:39], v[84:87]\n\t"
    "ds_read_b64_tr_b16 a[76:77], v239 offset:7264\n\t"
    "v_cvt_pk_bf16_f32 v50, v36, v37\n\t"
    "v_cvt_pk_bf16_f32 v49, v34, v35\n\t"
    "v_cvt_pk_bf16_f32 v51, v38, v39\n\t"
    "v_permlane16_swap_b32 v48, v50\n\t"
    "s_nop 0\n\t"
    "v_permlane16_swap_b32 v49, v51\n\t"
    "v_mfma_f32_16x16x32_bf16 v[84:87], a[92:95], a[52:55], v[84:87]\n\t"
    "buffer_store_dwordx4 v[48:51], v245, s[44:47], s36 offen offset:512 sc1\n\t"
    "v_mul_f32 v246, s62, v40\n\t"
    "v_mul_f32 v247, s62, v41\n\t"
    "v_mul_f32 v248, s62, v42\n\t"
    "v_mul_f32 v249, s62, v43\n\t"
    "v_mfma_f32_16x16x32_bf16 v[80:83], a[92:95], a[48:51], v[80:83]\n\t"
    "ds_read_b64_tr_b16 a[78:79], v240 offset:7264\n\t"
    "v_mul_f32 v250, s62, v44\n\t"
    "v_mul_f32 v251, s62, v45\n\t"
    "v_mul_f32 v252, s62, v46\n\t"
    "v_mul_f32 v253, s62, v47\n\t"
    "v_exp_f32 v246, v246\n\t"
    "v_mfma_f32_16x16x32_bf16 v[80:83], a[88:91], a[32:35], v[80:83]\n\t"
    "v_exp_f32 v247, v247\n\t"
    "v_exp_f32 v248, v248\n\t"
    "v_exp_f32 v249, v249\n\t"
    "s_waitcnt lgkmcnt(4)\n\t"
    "v_mfma_f32_16x16x32_bf16 v[96:99], a[64:67], a[0:3], v[96:99]\n\t"
    "ds_read_b64_tr_b16 a[80:81], v241 offset:14432\n\t"
    "v_exp_f32 v250, v250\n\t"
    "v_exp_f32 v251, v251\n\t"
    "v_exp_f32 v252, v252\n\t"
    "v_mfma_f32_16x16x32_bf16 v[96:99], a[68:71], a[16:19], v[96:99]\n\t"
    "v_exp_f32 v253, v253\n\t"
    "v_add_f32 v246, 1.0, v246\n\t"
    "v_add_f32 v247, 1.0, v247\n\t"
    "v_mfma_f32_16x16x32_bf16 v[100:103], a[68:71], a[20:23], v[100:103]\n\t"
    "ds_read_b64_tr_b16 a[82:83], v242 offset:14432\n\t"
    "v_add_f32 v248, 1.0, v248\n\t"
    "v_add_f32 v249, 1.0, v249\n\t"
    "v_add_f32 v250, 1.0, v250\n\t"
    "v_add_f32 v251, 1.0, v251\n\t"
    "v_add_f32 v252, 1.0, v252\n\t"
    "v_add_f32 v253, 1.0, v253\n\t"
    "v_mfma_f32_16x16x32_bf16 v[100:103], a[64:67], a[4:7], v[100:103]\n\t"
    "v_rcp_f32 v246, v246\n\t"
    "v_rcp_f32 v247, v247\n\t"
    "v_rcp_f32 v248, v248\n\t"
    "v_mfma_f32_16x16x32_bf16 v[104:107], a[64:67], a[8:11], v[104:107]\n\t"
    "ds_read_b64_tr_b16 a[84:85], v241 offset:21600\n\t"
    "v_rcp_f32 v249, v249\n\t"
    "v_rcp_f32 v250, v250\n\t"
    "v_rcp_f32 v251, v251\n\t"
    "v_mfma_f32_16x16x32_bf16 v[104:107], a[68:71], a[24:27], v[104:107]\n\t"
    "v_rcp_f32 v252, v252\n\t"
    "v_rcp_f32 v253, v253\n\t"
    "v_mfma_f32_16x16x32_bf16 v[108:111], a[68:71], a[28:31], v[108:111]\n\t"
    "ds_read_b64_tr_b16 a[86:87], v242 offset:21600\n\t"
    "v_mul_f32 v40, v246, v40\n\t"
    "v_mul_f32 v41, v247, v41\n\t"
    "v_mul_f32 v42, v248, v42\n\t"
    "v_mul_f32 v43, v249, v43\n\t"
    "v_mul_f32 v44, v250, v44\n\t"
    "v_mul_f32 v45, v251, v45\n\t"
    "v_mfma_f32_16x16x32_bf16 v[108:111], a[64:67], a[12:15], v[108:111]\n\t"
    "v_mul_f32 v46, v252, v46\n\t"
    "v_mul_f32 v47, v253, v47\n\t"
    "v_mul_f32 v40, v56, v40\n\t"
    "v_mul_f32 v41, v57, v41\n\t"
    "v_mul_f32 v42, v58, v42\n\t"
    "s_waitcnt lgkmcnt(4)\n\t"
    "v_mfma_f32_16x16x32_bf16 v[108:111], a[72:75], a[44:47], v[108:111]\n\t"
    "ds_read_b64_tr_b16 a[88:89], v239 offset:14432\n\t"
    "v_mul_f32 v43, v59, v43\n\t"
    "v_mul_f32 v44, v60, v44\n\t"
    "v_mul_f32 v45, v61, v45\n\t"
    "v_mul_f32 v46, v62, v46\n\t"
    "v_mul_f32 v47, v63, v47\n\t"
    "v_cvt_pk_bf16_f32 v56, v40, v41\n\t"
    "v_mfma_f32_16x16x32_bf16 v[108:111], a[76:79], a[60:63], v[108:111]\n\t"
    "v_cvt_pk_bf16_f32 v58, v44, v45\n\t"
    "v_cvt_pk_bf16_f32 v57, v42, v43\n\t"
    "v_cvt_pk_bf16_f32 v59, v46, v47\n\t"
    "v_permlane16_swap_b32 v56, v58\n\t"
    "s_nop 0\n\t"
    "v_mfma_f32_16x16x32_bf16 v[104:107], a[76:79], a[56:59], v[104:107]\n\t"
    "ds_read_b64_tr_b16 a[90:91], v240 offset:14432\n\t"
    "v_permlane16_swap_b32 v57, v59\n\t"
    "buffer_store_dwordx4 v[56:59], v245, s[44:47], s37 offen offset:512 sc1\n\t"
    "v_mul_f32 v246, s62, v64\n\t"
    "v_mul_f32 v247, s62, v65\n\t"
    "v_mul_f32 v248, s62, v66\n\t"
    "v_mul_f32 v249, s62, v67\n\t"
    "v_mfma_f32_16x16x32_bf16 v[104:107], a[72:75], a[40:43], v[104:107]\n\t"
    "v_mul_f32 v250, s62, v68\n\t"
    "v_mul_f32 v251, s62, v69\n\t"
    "v_mul_f32 v252, s62, v70\n\t"
    "v_mul_f32 v253, s62, v71\n\t"
    "v_exp_f32 v246, v246\n\t"
    "v_mfma_f32_16x16x32_bf16 v[100:103], a[72:75], a[36:39], v[100:103]\n\t"
    "ds_read_b64_tr_b16 a[92:93], v239 offset:21600\n\t"
    "v_exp_f32 v247, v247\n\t"
    "v_exp_f32 v248, v248\n\t"
    "v_exp_f32 v249, v249\n\t"
    "v_mfma_f32_16x16x32_bf16 v[100:103], a[76:79], a[52:55], v[100:103]\n\t"
    "v_exp_f32 v250, v250\n\t"
    "v_exp_f32 v251, v251\n\t"
    "v_mfma_f32_16x16x32_bf16 v[96:99], a[76:79], a[48:51], v[96:99]\n\t"
    "ds_read_b64_tr_b16 a[94:95], v240 offset:21600\n\t"
    "v_exp_f32 v252, v252\n\t"
    "v_exp_f32 v253, v253\n\t"
    "v_add_f32 v246, 1.0, v246\n\t"
    "v_add_f32 v247, 1.0, v247\n\t"
    "v_mfma_f32_16x16x32_bf16 v[96:99], a[72:75], a[32:35], v[96:99]\n\t"
    "v_add_f32 v248, 1.0, v248\n\t"
    "v_add_f32 v249, 1.0, v249\n\t"
    "v_add_f32 v250, 1.0, v250\n\t"
    "v_add_f32 v251, 1.0, v251\n\t"
    "v_add_f32 v252, 1.0, v252\n\t"
    "s_waitcnt lgkmcnt(4)\n\t"
    "v_mfma_f32_16x16x32_bf16 v[112:115], a[80:83], a[0:3], v[112:115]\n\t"
    "ds_read_b64_tr_b16 a[64:65], v241 offset:128\n\t"
    "v_add_f32 v253, 1.0, v253\n\t"
    "v_rcp_f32 v246, v246\n\t"
    "v_rcp_f32 v247, v247\n\t"
    "v_rcp_f32 v248, v248\n\t"
    "v_mfma_f32_16x16x32_bf16 v[112:115], a[84:87], a[16:19], v[112:115]\n\t"
    "v_rcp_f32 v249, v249\n\t"
    "v_rcp_f32 v250, v250\n\t"
    "v_mfma_f32_16x16x32_bf16 v[116:119], a[84:87], a[20:23], v[116:119]\n\t"
    "ds_read_b64_tr_b16 a[66:67], v242 offset:128\n\t"
    "v_rcp_f32 v251, v251\n\t"
    "v_rcp_f32 v252, v252\n\t"
    "v_rcp_f32 v253, v253\n\t"
    "v_mfma_f32_16x16x32_bf16 v[116:119], a[80:83], a[4:7], v[116:119]\n\t"
    "v_mul_f32 v64, v246, v64\n\t"
    "v_mul_f32 v65, v247, v65\n\t"
    "v_mul_f32 v66, v248, v66\n\t"
    "v_mul_f32 v67, v249, v67\n\t"
    "v_mul_f32 v68, v250, v68\n\t"
    "v_mfma_f32_16x16x32_bf16 v[120:123], a[80:83], a[8:11], v[120:123]\n\t"
    "ds_read_b64_tr_b16 a[68:69], v241 offset:7296\n\t"
    "v_mul_f32 v69, v251, v69\n\t"
    "v_mul_f32 v70, v252, v70\n\t"
    "v_mul_f32 v71, v253, v71\n\t"
    "v_mul_f32 v64, v80, v64\n\t"
    "v_mul_f32 v65, v81, v65\n\t"
    "v_mul_f32 v66, v82, v66\n\t"
    "v_mfma_f32_16x16x32_bf16 v[120:123], a[84:87], a[24:27], v[120:123]\n\t"
    "v_mul_f32 v67, v83, v67\n\t"
    "v_mul_f32 v68, v84, v68\n\t"
    "v_mul_f32 v69, v85, v69\n\t"
    "v_mul_f32 v70, v86, v70\n\t"
    "v_mul_f32 v71, v87, v71\n\t"
    "v_mfma_f32_16x16x32_bf16 v[124:127], a[84:87], a[28:31], v[124:127]\n\t"
    "ds_read_b64_tr_b16 a[70:71], v242 offset:7296\n\t"
    "v_cvt_pk_bf16_f32 v80, v64, v65\n\t"
    "v_cvt_pk_bf16_f32 v82, v68, v69\n\t"
    "v_cvt_pk_bf16_f32 v81, v66, v67\n\t"
    "v_cvt_pk_bf16_f32 v83, v70, v71\n\t"
    "v_permlane16_swap_b32 v80, v82\n\t"
    "s_nop 0\n\t"
    "v_mfma_f32_16x16x32_bf16 v[124:127], a[80:83], a[12:15], v[124:127]\n\t"
    "v_permlane16_swap_b32 v81, v83\n\t"
    "buffer_store_dwordx4 v[80:83], v245, s[44:47], s38 offen offset:0 sc1\n\t"
    "v_mul_f32 v246, s62, v72\n\t"
    "v_mul_f32 v247, s62, v73\n\t"
    "v_mul_f32 v248, s62, v74\n\t"
    "s_waitcnt lgkmcnt(4)\n\t"
    "v_mfma_f32_16x16x32_bf16 v[124:127], a[88:91], a[44:47], v[124:127]\n\t"
    "ds_read_b64_tr_b16 a[72:73], v239 offset:128\n\t"
    "v_mul_f32 v249, s62, v75\n\t"
    "v_mul_f32 v250, s62, v76\n\t"
    "v_mul_f32 v251, s62, v77\n\t"
    "v_mul_f32 v252, s62, v78\n\t"
    "v_mul_f32 v253, s62, v79\n\t"
    "v_exp_f32 v246, v246\n\t"
    "v_mfma_f32_16x16x32_bf16 v[124:127], a[92:95], a[60:63], v[124:127]\n\t"
    "v_exp_f32 v247, v247\n\t"
    "v_exp_f32 v248, v248\n\t"
    "v_mfma_f32_16x16x32_bf16 v[120:123], a[92:95], a[56:59], v[120:123]\n\t"
    "ds_read_b64_tr_b16 a[74:75], v240 offset:128\n\t"
    "v_exp_f32 v249, v249\n\t"
    "v_exp_f32 v250, v250\n\t"
    "v_exp_f32 v251, v251\n\t"
    "v_mfma_f32_16x16x32_bf16 v[120:123], a[88:91], a[40:43], v[120:123]\n\t"
    "v_exp_f32 v252, v252\n\t"
    "v_exp_f32 v253, v253\n\t"
    "v_add_f32 v246, 1.0, v246\n\t"
    "v_mfma_f32_16x16x32_bf16 v[116:119], a[88:91], a[36:39], v[116:119]\n\t"
    "ds_read_b64_tr_b16 a[76:77], v239 offset:7296\n\t"
    "v_add_f32 v247, 1.0, v247\n\t"
    "v_add_f32 v248, 1.0, v248\n\t"
    "v_add_f32 v249, 1.0, v249\n\t"
    "v_add_f32 v250, 1.0, v250\n\t"
    "v_add_f32 v251, 1.0, v251\n\t"
    "v_add_f32 v252, 1.0, v252\n\t"
    "v_mfma_f32_16x16x32_bf16 v[116:119], a[92:95], a[52:55], v[116:119]\n\t"
    "v_add_f32 v253, 1.0, v253\n\t"
    "v_rcp_f32 v246, v246\n\t"
    "v_rcp_f32 v247, v247\n\t"
    "v_mfma_f32_16x16x32_bf16 v[112:115], a[92:95], a[48:51], v[112:115]\n\t"
    "ds_read_b64_tr_b16 a[78:79], v240 offset:7296\n\t"
    "v_rcp_f32 v248, v248\n\t"
    "v_rcp_f32 v249, v249\n\t"
    "v_rcp_f32 v250, v250\n\t"
    "v_mfma_f32_16x16x32_bf16 v[112:115], a[88:91], a[32:35], v[112:115]\n\t"
    "v_rcp_f32 v251, v251\n\t"
    "v_rcp_f32 v252, v252\n\t"
    "v_rcp_f32 v253, v253\n\t"
    "s_waitcnt lgkmcnt(4)\n\t"
    "v_mfma_f32_16x16x32_bf16 v[128:131], a[64:67], a[0:3], v[128:131]\n\t"
    "ds_read_b64_tr_b16 a[80:81], v241 offset:14464\n\t"
    "v_mul_f32 v72, v246, v72\n\t"
    "v_mul_f32 v73, v247, v73\n\t"
    "v_mul_f32 v74, v248, v74\n\t"
    "v_mul_f32 v75, v249, v75\n\t"
    "v_mul_f32 v76, v250, v76\n\t"
    "s_waitcnt vmcnt(1)\n\t"
    "s_barrier\n\t"
    "buffer_store_dwordx2 v[232:233], v236, s[44:47], 0 offen sc1\n\t"
    "v_mfma_f32_16x16x32_bf16 v[128:131], a[68:71], a[16:19], v[128:131]\n\t"
    "v_mul_f32 v77, v251, v77\n\t"
    "v_mul_f32 v78, v252, v78\n\t"
    "v_mul_f32 v79, v253, v79\n\t"
    "v_mul_f32 v72, v88, v72\n\t"
    "v_mul_f32 v73, v89, v73\n\t"
    "v_mfma_f32_16x16x32_bf16 v[132:135], a[68:71], a[20:23], v[132:135]\n\t"
    "ds_read_b64_tr_b16 a[82:83], v242 offset:14464\n\t"
    "v_mul_f32 v74, v90, v74\n\t"
    "v_mul_f32 v75, v91, v75\n\t"
    "v_mul_f32 v76, v92, v76\n\t"
    "v_mul_f32 v77, v93, v77\n\t"
    "v_mul_f32 v78, v94, v78\n\t"
    "v_mul_f32 v79, v95, v79\n\t"
    "v_mfma_f32_16x16x32_bf16 v[132:135], a[64:67], a[4:7], v[132:135]\n\t"
    "v_cvt_pk_bf16_f32 v88, v72, v73\n\t"
    "v_cvt_pk_bf16_f32 v90, v76, v77\n\t"
    "v_cvt_pk_bf16_f32 v89, v74, v75\n\t"
    "v_cvt_pk_bf16_f32 v91, v78, v79\n\t"
    "v_permlane16_swap_b32 v88, v90\n\t"
    "v_mfma_f32_16x16x32_bf16 v[136:139], a[64:67], a[8:11], v[136:139]\n\t"
    "ds_read_b64_tr_b16 a[84:85], v241 offset:21632\n\t"
    "s_nop 0\n\t"
    "v_permlane16_swap_b32 v89, v91\n\t"
    "buffer_store_dwordx4 v[88:91], v245, s[44:47], s39 offen offset:0 sc1\n\t"
    "v_mfma_f32_16x16x32_bf16 v[136:139], a[68:71], a[24:27], v[136:139]\n\t"
    "v_mfma_f32_16x16x32_bf16 v[140:143], a[68:71], a[28:31], v[140:143]\n\t"
    "ds_read_b64_tr_b16 a[86:87], v242 offset:21632\n\t"
    "v_mul_f32 v246, s62, v96\n\t"
    "v_mul_f32 v247, s62, v97\n\t"
    "v_mul_f32 v248, s62, v98\n\t"
    "v_mul_f32 v249, s62, v99\n\t"
    "v_mul_f32 v250, s62, v100\n\t"
    "v_mul_f32 v251, s62, v101\n\t"
    "v_mfma_f32_16x16x32_bf16 v[140:143], a[64:67], a[12:15], v[140:143]\n\t"
    "v_mul_f32 v252, s62, v102\n\t"
    "v_mul_f32 v253, s62, v103\n\t"
    "v_exp_f32 v246, v246\n\t"
    "v_exp_f32 v247, v247\n\t"
    "s_waitcnt lgkmcnt(4)\n\t"
    "v_mfma_f32_16x16x32_bf16 v[140:143], a[72:75], a[44:47], v[140:143]\n\t"
    "ds_read_b64_tr_b16 a[88:89], v239 offset:14464\n\t"
    "v_exp_f32 v248, v248\n\t"
    "v_exp_f32 v249, v249\n\t"
    "v_exp_f32 v250, v250\n\t"
    "v_mfma_f32_16x16x32_bf16 v[140:143], a[76:79], a[60:63], v[140:143]\n\t"
    "v_exp_f32 v251, v251\n\t"
    "v_exp_f32 v252, v252\n\t"
    "v_mfma_f32_16x16x32_bf16 v[136:139], a[76:79], a[56:59], v[136:139]\n\t"
    "ds_read_b64_tr_b16 a[90:91], v240 offset:14464\n\t"
    "v_exp_f32 v253, v253\n\t"
    "v_add_f32 v246, 1.0, v246\n\t"
    "v_add_f32 v247, 1.0, v247\n\t"
    "v_add_f32 v248, 1.0, v248\n\t"
    "v_add_f32 v249, 1.0, v249\n\t"
    "v_mfma_f32_16x16x32_bf16 v[136:139], a[72:75], a[40:43], v[136:139]\n\t"
    "v_add_f32 v250, 1.0, v250\n\t"
    "v_add_f32 v251, 1.0, v251\n\t"
    "v_add_f32 v252, 1.0, v252\n\t"
    "v_add_f32 v253, 1.0, v253\n\t"
    "v_rcp_f32 v246, v246\n\t"
    "v_mfma_f32_16x16x32_bf16 v[132:135], a[72:75], a[36:39], v[132:135]\n\t"
    "ds_read_b64_tr_b16 a[92:93], v239 offset:21632\n\t"
    "v_rcp_f32 v247, v247\n\t"
    "v_rcp_f32 v248, v248\n\t"
    "v_rcp_f32 v249, v249\n\t"
    "v_mfma_f32_16x16x32_bf16 v[132:135], a[76:79], a[52:55], v[132:135]\n\t"
    "v_rcp_f32 v250, v250\n\t"
    "v_rcp_f32 v251, v251\n\t"
    "v_mfma_f32_16x16x32_bf16 v[128:131], a[76:79], a[48:51], v[128:131]\n\t"
    "ds_read_b64_tr_b16 a[94:95], v240 offset:21632\n\t"
    "v_rcp_f32 v252, v252\n\t"
    "v_rcp_f32 v253, v253\n\t"
    "v_mul_f32 v96, v246, v96\n\t"
    "v_mul_f32 v97, v247, v97\n\t"
    "v_mfma_f32_16x16x32_bf16 v[128:131], a[72:75], a[32:35], v[128:131]\n\t"
    "v_mul_f32 v98, v248, v98\n\t"
    "v_mul_f32 v99, v249, v99\n\t"
    "v_mul_f32 v100, v250, v100\n\t"
    "v_mul_f32 v101, v251, v101\n\t"
    "v_mul_f32 v102, v252, v102\n\t"
    "s_waitcnt lgkmcnt(4)\n\t"
    "v_mfma_f32_16x16x32_bf16 v[144:147], a[80:83], a[0:3], v[144:147]\n\t"
    "ds_read_b64_tr_b16 a[64:65], v241 offset:160\n\t"
    "v_mul_f32 v103, v253, v103\n\t"
    "v_mul_f32 v96, v112, v96\n\t"
    "v_mul_f32 v97, v113, v97\n\t"
    "v_mul_f32 v98, v114, v98\n\t"
    "v_mul_f32 v99, v115, v99\n\t"
    "v_mul_f32 v100, v116, v100\n\t"
    "v_mfma_f32_16x16x32_bf16 v[144:147], a[84:87], a[16:19], v[144:147]\n\t"
    "v_mul_f32 v101, v117, v101\n\t"
    "v_mul_f32 v102, v118, v102\n\t"
    "v_mul_f32 v103, v119, v103\n\t"
    "v_cvt_pk_bf16_f32 v112, v96, v97\n\t"
    "v_cvt_pk_bf16_f32 v114, v100, v101\n\t"
    "v_mfma_f32_16x16x32_bf16 v[148:151], a[84:87], a[20:23], v[148:151]\n\t"
    "ds_read_b64_tr_b16 a[66:67], v242 offset:160\n\t"
    "v_cvt_pk_bf16_f32 v113, v98, v99\n\t"
    "v_cvt_pk_bf16_f32 v115, v102, v103\n\t"
    "v_permlane16_swap_b32 v112, v114\n\t"
    "s_nop 0\n\t"
    "v_permlane16_swap_b32 v113, v115\n\t"
    "buffer_store_dwordx4 v[112:115], v245, s[44:47], s38 offen offset:512 sc1\n\t"
    "v_mfma_f32_16x16x32_bf16 v[148:151], a[80:83], a[4:7], v[148:151]\n\t"
    "v_mul_f32 v246, s62, v104\n\t"
    "v_mul_f32 v247, s62, v105\n\t"
    "v_mul_f32 v248, s62, v106\n\t"
    "v_mul_f32 v249, s62, v107\n\t"
    "v_mul_f32 v250, s62, v108\n\t"
    "v_mfma_f32_16x16x32_bf16 v[152:155], a[80:83], a[8:11], v[152:155]\n\t"
    "ds_read_b64_tr_b16 a[68:69], v241 offset:7328\n\t"
    "v_mul_f32 v251, s62, v109\n\t"
    "v_mul_f32 v252, s62, v110\n\t"
    "v_mul_f32 v253, s62, v111\n\t"
    "v_exp_f32 v246, v246\n\t"
    "v_exp_f32 v247, v247\n\t"
    "v_mfma_f32_16x16x32_bf16 v[152:155], a[84:87], a[24:27], v[152:155]\n\t"
    "v_exp_f32 v248, v248\n\t"
    "v_exp_f32 v249, v249\n\t"
    "v_mfma_f32_16x16x32_bf16 v[156:159], a[84:87], a[28:31], v[156:159]\n\t"
    "ds_read_b64_tr_b16 a[70:71], v242 offset:7328\n\t"
    "v_exp_f32 v250, v250\n\t"
    "v_exp_f32 v251, v251\n\t"
    "v_exp_f32 v252, v252\n\t"
    "v_mfma_f32_16x16x32_bf16 v[156:159], a[80:83], a[12:15], v[156:159]\n\t"
    "v_exp_f32 v253, v253\n\t"
    "v_add_f32 v246, 1.0, v246\n\t"
    "v_add_f32 v247, 1.0, v247\n\t"
    "v_add_f32 v248, 1.0, v248\n\t"
    "s_waitcnt lgkmcnt(4)\n\t"
    "v_mfma_f32_16x16x32_bf16 v[156:159], a[88:91], a[44:47], v[156:159]\n\t"
    "ds_read_b64_tr_b16 a[72:73], v239 offset:160\n\t"
    "v_add_f32 v249, 1.0, v249\n\t"
    "v_add_f32 v250, 1.0, v250\n\t"
    "v_add_f32 v251, 1.0, v251\n\t"
    "v_add_f32 v252, 1.0, v252\n\t"
    "v_add_f32 v253, 1.0, v253\n\t"
    "v_rcp_f32 v246, v246\n\t"
    "v_mfma_f32_16x16x32_bf16 v[156:159], a[92:95], a[60:63], v[156:159]\n\t"
    "v_rcp_f32 v247, v247\n\t"
    "v_rcp_f32 v248, v248\n\t"
    "v_mfma_f32_16x16x32_bf16 v[152:155], a[92:95], a[56:59], v[152:155]\n\t"
    "ds_read_b64_tr_b16 a[74:75], v240 offset:160\n\t"
    "v_rcp_f32 v249, v249\n\t"
    "v_rcp_f32 v250, v250\n\t"
    "v_rcp_f32 v251, v251\n\t"
    "v_mfma_f32_16x16x32_bf16 v[152:155], a[88:91], a[40:43], v[152:155]\n\t"
    "v_rcp_f32 v252, v252\n\t"
    "v_rcp_f32 v253, v253\n\t"
    "v_mul_f32 v104, v246, v104\n\t"
    "v_mfma_f32_16x16x32_bf16 v[148:151], a[88:91], a[36:39], v[148:151]\n\t"
    "ds_read_b64_tr_b16 a[76:77], v239 offset:7328\n\t"
    "v_mul_f32 v105, v247, v105\n\t"
    "v_mul_f32 v106, v248, v106\n\t"
    "v_mul_f32 v107, v249, v107\n\t"
    "v_mul_f32 v108, v250, v108\n\t"
    "v_mul_f32 v109, v251, v109\n\t"
    "v_mul_f32 v110, v252, v110\n\t"
    "v_mfma_f32_16x16x32_bf16 v[148:151], a[92:95], a[52:55], v[148:151]\n\t"
    "v_mul_f32 v111, v253, v111\n\t"
    "v_mul_f32 v104, v120, v104\n\t"
    "v_mul_f32 v105, v121, v105\n\t"
    "v_mul_f32 v106, v122, v106\n\t"
    "v_mul_f32 v107, v123, v107\n\t"
    "v_mfma_f32_16x16x32_bf16 v[144:147], a[92:95], a[48:51], v[144:147]\n\t"
    "ds_read_b64_tr_b16 a[78:79], v240 offset:7328\n\t"
    "v_mul_f32 v108, v124, v108\n\t"
    "v_mul_f32 v109, v125, v109\n\t"
    "v_mul_f32 v110, v126, v110\n\t"
    "v_mul_f32 v111, v127, v111\n\t"
    "v_cvt_pk_bf16_f32 v120, v104, v105\n\t"
    "v_cvt_pk_bf16_f32 v122, v108, v109\n\t"
    "v_mfma_f32_16x16x32_bf16 v[144:147], a[88:91], a[32:35], v[144:147]\n\t"
    "v_cvt_pk_bf16_f32 v121, v106, v107\n\t"
    "v_cvt_pk_bf16_f32 v123, v110, v111\n\t"
    "v_permlane16_swap_b32 v120, v122\n\t"
    "s_nop 0\n\t"
    "v_permlane16_swap_b32 v121, v123\n\t"
    "s_waitcnt lgkmcnt(4)\n\t"
    "v_mfma_f32_16x16x32_bf16 v[160:163], a[64:67], a[0:3], v[160:163]\n\t"
    "ds_read_b64_tr_b16 a[80:81], v241 offset:14496\n\t"
    "buffer_store_dwordx4 v[120:123], v245, s[44:47], s39 offen offset:512 sc1\n\t"
    "v_mfma_f32_16x16x32_bf16 v[160:163], a[68:71], a[16:19], v[160:163]\n\t"
    "v_mfma_f32_16x16x32_bf16 v[164:167], a[68:71], a[20:23], v[164:167]\n\t"
    "ds_read_b64_tr_b16 a[82:83], v242 offset:14496\n\t"
    "v_mfma_f32_16x16x32_bf16 v[164:167], a[64:67], a[4:7], v[164:167]\n\t"
    "v_mfma_f32_16x16x32_bf16 v[168:171], a[64:67], a[8:11], v[168:171]\n\t"
    "ds_read_b64_tr_b16 a[84:85], v241 offset:21664\n\t"
    "v_mfma_f32_16x16x32_bf16 v[168:171], a[68:71], a[24:27], v[168:171]\n\t"
    "v_mfma_f32_16x16x32_bf16 v[172:175], a[68:71], a[28:31], v[172:175]\n\t"
    "ds_read_b64_tr_b16 a[86:87], v242 offset:21664\n\t"
    "v_mul_f32 v246, s62, v128\n\t"
    "v_mul_f32 v247, s62, v129\n\t"
    "v_mul_f32 v248, s62, v130\n\t"
    "v_mul_f32 v249, s62, v131\n\t"
    "v_mul_f32 v250, s62, v132\n\t"
    "v_mul_f32 v251, s62, v133\n\t"
    "v_mfma_f32_16x16x32_bf16 v[172:175], a[64:67], a[12:15], v[172:175]\n\t"
    "v_mul_f32 v252, s62, v134\n\t"
    "v_mul_f32 v253, s62, v135\n\t"
    "v_exp_f32 v246, v246\n\t"
    "v_exp_f32 v247, v247\n\t"
    "s_waitcnt lgkmcnt(4)\n\t"
    "v_mfma_f32_16x16x32_bf16 v[172:175], a[72:75], a[44:47], v[172:175]\n\t"
    "ds_read_b64_tr_b16 a[88:89], v239 offset:14496\n\t"
    "v_exp_f32 v248, v248\n\t"
    "v_exp_f32 v249, v249\n\t"
    "v_exp_f32 v250, v250\n\t"
    "v_mfma_f32_16x16x32_bf16 v[172:175], a[76:79], a[60:63], v[172:175]\n\t"
    "v_exp_f32 v251, v251\n\t"
    "v_exp_f32 v252, v252\n\t"
    "v_mfma_f32_16x16x32_bf16 v[168:171], a[76:79], a[56:59], v[168:171]\n\t"
    "ds_read_b64_tr_b16 a[90:91], v240 offset:14496\n\t"
    "v_exp_f32 v253, v253\n\t"
    "v_add_f32 v246, 1.0, v246\n\t"
    "v_add_f32 v247, 1.0, v247\n\t"
    "v_add_f32 v248, 1.0, v248\n\t"
    "v_add_f32 v249, 1.0, v249\n\t"
    "v_mfma_f32_16x16x32_bf16 v[168:171], a[72:75], a[40:43], v[168:171]\n\t"
    "v_add_f32 v250, 1.0, v250\n\t"
    "v_add_f32 v251, 1.0, v251\n\t"
    "v_add_f32 v252, 1.0, v252\n\t"
    "v_add_f32 v253, 1.0, v253\n\t"
    "v_rcp_f32 v246, v246\n\t"
    "v_mfma_f32_16x16x32_bf16 v[164:167], a[72:75], a[36:39], v[164:167]\n\t"
    "ds_read_b64_tr_b16 a[92:93], v239 offset:21664\n\t"
    "v_rcp_f32 v247, v247\n\t"
    "v_rcp_f32 v248, v248\n\t"
    "v_rcp_f32 v249, v249\n\t"
    "v_mfma_f32_16x16x32_bf16 v[164:167], a[76:79], a[52:55], v[164:167]\n\t"
    "v_rcp_f32 v250, v250\n\t"
    "v_rcp_f32 v251, v251\n\t"
    "v_mfma_f32_16x16x32_bf16 v[160:163], a[76:79], a[48:51], v[160:163]\n\t"
    "ds_read_b64_tr_b16 a[94:95], v240 offset:21664\n\t"
    "v_rcp_f32 v252, v252\n\t"
    "v_rcp_f32 v253, v253\n\t"
    "v_mul_f32 v128, v246, v128\n\t"
    "v_mul_f32 v129, v247, v129\n\t"
    "v_mfma_f32_16x16x32_bf16 v[160:163], a[72:75], a[32:35], v[160:163]\n\t"
    "v_mul_f32 v130, v248, v130\n\t"
    "v_mul_f32 v131, v249, v131\n\t"
    "v_mul_f32 v132, v250, v132\n\t"
    "v_mul_f32 v133, v251, v133\n\t"
    "v_mul_f32 v134, v252, v134\n\t"
    "s_waitcnt lgkmcnt(4)\n\t"
    "v_mfma_f32_16x16x32_bf16 v[176:179], a[80:83], a[0:3], v[176:179]\n\t"
    "ds_read_b64_tr_b16 a[64:65], v241 offset:192\n\t"
    "v_mul_f32 v135, v253, v135\n\t"
    "v_mul_f32 v128, v144, v128\n\t"
    "v_mul_f32 v129, v145, v129\n\t"
    "v_mul_f32 v130, v146, v130\n\t"
    "v_mul_f32 v131, v147, v131\n\t"
    "v_mul_f32 v132, v148, v132\n\t"
    "v_mfma_f32_16x16x32_bf16 v[176:179], a[84:87], a[16:19], v[176:179]\n\t"
    "v_mul_f32 v133, v149, v133\n\t"
    "v_mul_f32 v134, v150, v134\n\t"
    "v_mul_f32 v135, v151, v135\n\t"
    "v_cvt_pk_bf16_f32 v144, v128, v129\n\t"
    "v_cvt_pk_bf16_f32 v146, v132, v133\n\t"
    "v_mfma_f32_16x16x32_bf16 v[180:183], a[84:87], a[20:23], v[180:183]\n\t"
    "ds_read_b64_tr_b16 a[66:67], v242 offset:192\n\t"
    "v_cvt_pk_bf16_f32 v145, v130, v131\n\t"
    "v_cvt_pk_bf16_f32 v147, v134, v135\n\t"
    "v_permlane16_swap_b32 v144, v146\n\t"
    "s_nop 0\n\t"
    "v_permlane16_swap_b32 v145, v147\n\t"
    "buffer_store_dwordx4 v[144:147], v245, s[44:47], s38 offen offset:1024 sc1\n\t"
    "v_mfma_f32_16x16x32_bf16 v[180:183], a[80:83], a[4:7], v[180:183]\n\t"
    "v_mul_f32 v246, s62, v136\n\t"
    "v_mul_f32 v247, s62, v137\n\t"
    "v_mul_f32 v248, s62, v138\n\t"
    "v_mul_f32 v249, s62, v139\n\t"
    "v_mul_f32 v250, s62, v140\n\t"
    "v_mfma_f32_16x16x32_bf16 v[184:187], a[80:83], a[8:11], v[184:187]\n\t"
    "ds_read_b64_tr_b16 a[68:69], v241 offset:7360\n\t"
    "v_mul_f32 v251, s62, v141\n\t"
    "v_mul_f32 v252, s62, v142\n\t"
    "v_mul_f32 v253, s62, v143\n\t"
    "v_exp_f32 v246, v246\n\t"
    "v_exp_f32 v247, v247\n\t"
    "v_mfma_f32_16x16x32_bf16 v[184:187], a[84:87], a[24:27], v[184:187]\n\t"
    "v_exp_f32 v248, v248\n\t"
    "v_exp_f32 v249, v249\n\t"
    "v_mfma_f32_16x16x32_bf16 v[188:191], a[84:87], a[28:31], v[188:191]\n\t"
    "ds_read_b64_tr_b16 a[70:71], v242 offset:7360\n\t"
    "v_exp_f32 v250, v250\n\t"
    "v_exp_f32 v251, v251\n\t"
    "v_exp_f32 v252, v252\n\t"
    "v_mfma_f32_16x16x32_bf16 v[188:191], a[80:83], a[12:15], v[188:191]\n\t"
    "v_exp_f32 v253, v253\n\t"
    "v_add_f32 v246, 1.0, v246\n\t"
    "v_add_f32 v247, 1.0, v247\n\t"
    "v_add_f32 v248, 1.0, v248\n\t"
    "s_waitcnt lgkmcnt(4)\n\t"
    "v_mfma_f32_16x16x32_bf16 v[188:191], a[88:91], a[44:47], v[188:191]\n\t"
    "ds_read_b64_tr_b16 a[72:73], v239 offset:192\n\t"
    "v_add_f32 v249, 1.0, v249\n\t"
    "v_add_f32 v250, 1.0, v250\n\t"
    "v_add_f32 v251, 1.0, v251\n\t"
    "v_add_f32 v252, 1.0, v252\n\t"
    "v_add_f32 v253, 1.0, v253\n\t"
    "v_rcp_f32 v246, v246\n\t"
    "v_mfma_f32_16x16x32_bf16 v[188:191], a[92:95], a[60:63], v[188:191]\n\t"
    "v_rcp_f32 v247, v247\n\t"
    "v_rcp_f32 v248, v248\n\t"
    "v_mfma_f32_16x16x32_bf16 v[184:187], a[92:95], a[56:59], v[184:187]\n\t"
    "ds_read_b64_tr_b16 a[74:75], v240 offset:192\n\t"
    "v_rcp_f32 v249, v249\n\t"
    "v_rcp_f32 v250, v250\n\t"
    "v_rcp_f32 v251, v251\n\t"
    "v_mfma_f32_16x16x32_bf16 v[184:187], a[88:91], a[40:43], v[184:187]\n\t"
    "v_rcp_f32 v252, v252\n\t"
    "v_rcp_f32 v253, v253\n\t"
    "v_mul_f32 v136, v246, v136\n\t"
    "v_mfma_f32_16x16x32_bf16 v[180:183], a[88:91], a[36:39], v[180:183]\n\t"
    "ds_read_b64_tr_b16 a[76:77], v239 offset:7360\n\t"
    "v_mul_f32 v137, v247, v137\n\t"
    "v_mul_f32 v138, v248, v138\n\t"
    "v_mul_f32 v139, v249, v139\n\t"
    "v_mul_f32 v140, v250, v140\n\t"
    "v_mul_f32 v141, v251, v141\n\t"
    "v_mul_f32 v142, v252, v142\n\t"
    "v_mfma_f32_16x16x32_bf16 v[180:183], a[92:95], a[52:55], v[180:183]\n\t"
    "v_mul_f32 v143, v253, v143\n\t"
    "v_mul_f32 v136, v152, v136\n\t"
    "v_mul_f32 v137, v153, v137\n\t"
    "v_mul_f32 v138, v154, v138\n\t"
    "v_mul_f32 v139, v155, v139\n\t"
    "v_mfma_f32_16x16x32_bf16 v[176:179], a[92:95], a[48:51], v[176:179]\n\t"
    "ds_read_b64_tr_b16 a[78:79], v240 offset:7360\n\t"
    "buffer_load_dwordx2 v[234:235], v237, s[44:47], 0 offen offset:0 sc1\n\t"
    "v_mul_f32 v140, v156, v140\n\t"
    "v_mul_f32 v141, v157, v141\n\t"
    "v_mul_f32 v142, v158, v142\n\t"
    "v_mul_f32 v143, v159, v143\n\t"
    "v_cvt_pk_bf16_f32 v152, v136, v137\n\t"
    "v_cvt_pk_bf16_f32 v154, v140, v141\n\t"
    "v_mfma_f32_16x16x32_bf16 v[176:179], a[88:91], a[32:35], v[176:179]\n\t"
    "v_cvt_pk_bf16_f32 v153, v138, v139\n\t"
    "v_cvt_pk_bf16_f32 v155, v142, v143\n\t"
    "v_permlane16_swap_b32 v152, v154\n\t"
    "s_nop 0\n\t"
    "v_permlane16_swap_b32 v153, v155\n\t"
    "s_waitcnt lgkmcnt(4)\n\t"
    "v_mfma_f32_16x16x32_bf16 v[192:195], a[64:67], a[0:3], v[192:195]\n\t"
    "ds_read_b64_tr_b16 a[80:81], v241 offset:14528\n\t"
    "buffer_store_dwordx4 v[152:155], v245, s[44:47], s39 offen offset:1024 sc1\n\t"
    "v_mfma_f32_16x16x32_bf16 v[192:195], a[68:71], a[16:19], v[192:195]\n\t"
    "v_mfma_f32_16x16x32_bf16 v[196:199], a[68:71], a[20:23], v[196:199]\n\t"
    "ds_read_b64_tr_b16 a[82:83], v242 offset:14528\n\t"
    "v_mfma_f32_16x16x32_bf16 v[196:199], a[64:67], a[4:7], v[196:199]\n\t"
    "v_mfma_f32_16x16x32_bf16 v[200:203], a[64:67], a[8:11], v[200:203]\n\t"
    "ds_read_b64_tr_b16 a[84:85], v241 offset:21696\n\t"
    "v_mfma_f32_16x16x32_bf16 v[200:203], a[68:71], a[24:27], v[200:203]\n\t"
    "v_mfma_f32_16x16x32_bf16 v[204:207], a[68:71], a[28:31], v[204:207]\n\t"
    "ds_read_b64_tr_b16 a[86:87], v242 offset:21696\n\t"
    "v_mul_f32 v246, s62, v160\n\t"
    "v_mul_f32 v247, s62, v161\n\t"
    "v_mul_f32 v248, s62, v162\n\t"
    "v_mul_f32 v249, s62, v163\n\t"
    "v_mul_f32 v250, s62, v164\n\t"
    "v_mul_f32 v251, s62, v165\n\t"
    "v_mfma_f32_16x16x32_bf16 v[204:207], a[64:67], a[12:15], v[204:207]\n\t"
    "v_mul_f32 v252, s62, v166\n\t"
    "v_mul_f32 v253, s62, v167\n\t"
    "v_exp_f32 v246, v246\n\t"
    "v_exp_f32 v247, v247\n\t"
    "s_waitcnt lgkmcnt(4)\n\t"
    "v_mfma_f32_16x16x32_bf16 v[204:207], a[72:75], a[44:47], v[204:207]\n\t"
    "ds_read_b64_tr_b16 a[88:89], v239 offset:14528\n\t"
    "v_exp_f32 v248, v248\n\t"
    "v_exp_f32 v249, v249\n\t"
    "v_exp_f32 v250, v250\n\t"
    "v_mfma_f32_16x16x32_bf16 v[204:207], a[76:79], a[60:63], v[204:207]\n\t"
    "v_exp_f32 v251, v251\n\t"
    "v_exp_f32 v252, v252\n\t"
    "v_mfma_f32_16x16x32_bf16 v[200:203], a[76:79], a[56:59], v[200:203]\n\t"
    "ds_read_b64_tr_b16 a[90:91], v240 offset:14528\n\t"
    "v_exp_f32 v253, v253\n\t"
    "v_add_f32 v246, 1.0, v246\n\t"
    "v_add_f32 v247, 1.0, v247\n\t"
    "v_add_f32 v248, 1.0, v248\n\t"
    "v_add_f32 v249, 1.0, v249\n\t"
    "v_mfma_f32_16x16x32_bf16 v[200:203], a[72:75], a[40:43], v[200:203]\n\t"
    "v_add_f32 v250, 1.0, v250\n\t"
    "v_add_f32 v251, 1.0, v251\n\t"
    "v_add_f32 v252, 1.0, v252\n\t"
    "v_add_f32 v253, 1.0, v253\n\t"
    "v_rcp_f32 v246, v246\n\t"
    "v_mfma_f32_16x16x32_bf16 v[196:199], a[72:75], a[36:39], v[196:199]\n\t"
    "ds_read_b64_tr_b16 a[92:93], v239 offset:21696\n\t"
    "v_rcp_f32 v247, v247\n\t"
    "v_rcp_f32 v248, v248\n\t"
    "v_rcp_f32 v249, v249\n\t"
    "v_mfma_f32_16x16x32_bf16 v[196:199], a[76:79], a[52:55], v[196:199]\n\t"
    "v_rcp_f32 v250, v250\n\t"
    "v_rcp_f32 v251, v251\n\t"
    "v_mfma_f32_16x16x32_bf16 v[192:195], a[76:79], a[48:51], v[192:195]\n\t"
    "ds_read_b64_tr_b16 a[94:95], v240 offset:21696\n\t"
    "v_rcp_f32 v252, v252\n\t"
    "v_rcp_f32 v253, v253\n\t"
    "v_mul_f32 v160, v246, v160\n\t"
    "v_mul_f32 v161, v247, v161\n\t"
    "v_mfma_f32_16x16x32_bf16 v[192:195], a[72:75], a[32:35], v[192:195]\n\t"
    "v_mul_f32 v162, v248, v162\n\t"
    "v_mul_f32 v163, v249, v163\n\t"
    "v_mul_f32 v164, v250, v164\n\t"
    "v_mul_f32 v165, v251, v165\n\t"
    "v_mul_f32 v166, v252, v166\n\t"
    "s_waitcnt lgkmcnt(4)\n\t"
    "v_mfma_f32_16x16x32_bf16 v[208:211], a[80:83], a[0:3], v[208:211]\n\t"
    "v_mul_f32 v167, v253, v167\n\t"
    "v_mul_f32 v160, v176, v160\n\t"
    "v_mul_f32 v161, v177, v161\n\t"
    "v_mul_f32 v162, v178, v162\n\t"
    "v_mul_f32 v163, v179, v163\n\t"
    "v_mul_f32 v164, v180, v164\n\t"
    "v_mfma_f32_16x16x32_bf16 v[208:211], a[84:87], a[16:19], v[208:211]\n\t"
    "v_mul_f32 v165, v181, v165\n\t"
    "v_mul_f32 v166, v182, v166\n\t"
    "v_mul_f32 v167, v183, v167\n\t"
    "v_cvt_pk_bf16_f32 v176, v160, v161\n\t"
    "v_cvt_pk_bf16_f32 v178, v164, v165\n\t"
    "v_mfma_f32_16x16x32_bf16 v[212:215], a[84:87], a[20:23], v[212:215]\n\t"
    "v_cvt_pk_bf16_f32 v177, v162, v163\n\t"
    "v_cvt_pk_bf16_f32 v179, v166, v167\n\t"
    "v_permlane16_swap_b32 v176, v178\n\t"
    "s_nop 0\n\t"
    "v_permlane16_swap_b32 v177, v179\n\t"
    "buffer_store_dwordx4 v[176:179], v245, s[44:47], s38 offen offset:1536 sc1\n\t"
    "v_mfma_f32_16x16x32_bf16 v[212:215], a[80:83], a[4:7], v[212:215]\n\t"
    "v_mul_f32 v246, s62, v168\n\t"
    "v_mul_f32 v247, s62, v169\n\t"
    "v_mul_f32 v248, s62, v170\n\t"
    "v_mul_f32 v249, s62, v171\n\t"
    "v_mul_f32 v250, s62, v172\n\t"
    "v_mfma_f32_16x16x32_bf16 v[216:219], a[80:83], a[8:11], v[216:219]\n\t"
    "v_mul_f32 v251, s62, v173\n\t"
    "v_mul_f32 v252, s62, v174\n\t"
    "v_mul_f32 v253, s62, v175\n\t"
    "v_exp_f32 v246, v246\n\t"
    "v_accvgpr_read_b32 v0, a228\n\t"
    "v_accvgpr_read_b32 v4, a233\n\t"
    "v_add_u32 v1, 114816, v0\n\t"
    "v_add_u32 v2, 229632, v0\n\t"
    "v_add_u32 v3, 344448, v0\n\t"
    "v_xor_b32 v5, 32, v4\n\t"
    "v_xor_b32 v6, 64, v4\n\t"
    "v_xor_b32 v7, 96, v4\n\t"
    "s_lshl1_add_u32 s59, s82, 0\n\t"
    "s_waitcnt vmcnt(2)\n\t"
    "v_cmp_ne_u64 vcc, v[234:235], v[232:233]\n\t"
    "s_nop 0\n\t"
    "s_cmp_eq_u64 vcc, 0\n\t"
    "s_cbranch_scc1 POLL1OK_%=\n\t"
    "s_mov_b32 s83, 0x40000\n\t"
    "POLL1_%=:\n\t"
    "buffer_load_dwordx2 v[234:235], v237, s[44:47], 0 offen offset:0 sc1\n\t"
    "s_waitcnt vmcnt(0)\n\t"
    "v_cmp_ne_u64 vcc, v[234:235], v[232:233]\n\t"
    "s_sub_u32 s83, s83, 1\n\t"
    "s_cmp_eq_u64 vcc, 0\n\t"
    "s_cbranch_scc1 POLL1OK_%=\n\t"
    "s_sleep 2\n\t"
    "s_cmp_lg_u32 s83, 0\n\t"
    "s_cbranch_scc1 POLL1_%=\n\t"
    "POLL1OK_%=:\n\t"
    "buffer_load_dwordx4 a[96:99], v0, s[44:47], s59 offen offset:0 sc1\n\t"
    "v_exp_f32 v247, v247\n\t"
    "v_mfma_f32_16x16x32_bf16 v[216:219], a[84:87], a[24:27], v[216:219]\n\t"
    "v_exp_f32 v248, v248\n\t"
    "v_exp_f32 v249, v249\n\t"
    "v_mfma_f32_16x16x32_bf16 v[220:223], a[84:87], a[28:31], v[220:223]\n\t"
    "v_exp_f32 v250, v250\n\t"
    "v_exp_f32 v251, v251\n\t"
    "v_exp_f32 v252, v252\n\t"
    "v_mfma_f32_16x16x32_bf16 v[220:223], a[80:83], a[12:15], v[220:223]\n\t"
    "v_exp_f32 v253, v253\n\t"
    "v_add_f32 v246, 1.0, v246\n\t"
    "buffer_load_dwordx4 a[100:103], v1, s[44:47], s59 offen offset:0 sc1\n\t"
    "v_add_f32 v247, 1.0, v247\n\t"
    "v_add_f32 v248, 1.0, v248\n\t"
    "s_waitcnt lgkmcnt(0)\n\t"
    "v_mfma_f32_16x16x32_bf16 v[220:223], a[88:91], a[44:47], v[220:223]\n\t"
    "v_add_f32 v249, 1.0, v249\n\t"
    "v_add_f32 v250, 1.0, v250\n\t"
    "v_add_f32 v251, 1.0, v251\n\t"
    "v_add_f32 v252, 1.0, v252\n\t"
    "v_add_f32 v253, 1.0, v253\n\t"
    "v_rcp_f32 v246, v246\n\t"
    "v_mfma_f32_16x16x32_bf16 v[220:223], a[92:95], a[60:63], v[220:223]\n\t"
    "buffer_load_dwordx4 a[104:107], v2, s[44:47], s59 offen offset:0 sc1\n\t"
    "v_rcp_f32 v247, v247\n\t"
    "v_rcp_f32 v248, v248\n\t"
    "v_mfma_f32_16x16x32_bf16 v[216:219], a[92:95], a[56:59], v[216:219]\n\t"
    "v_rcp_f32 v249, v249\n\t"
    "v_rcp_f32 v250, v250\n\t"
    "v_rcp_f32 v251, v251\n\t"
    "v_mfma_f32_16x16x32_bf16 v[216:219], a[88:91], a[40:43], v[216:219]\n\t"
    "v_rcp_f32 v252, v252\n\t"
    "v_rcp_f32 v253, v253\n\t"
    "v_mul_f32 v168, v246, v168\n\t"
    "v_mfma_f32_16x16x32_bf16 v[212:215], a[88:91], a[36:39], v[212:215]\n\t"
    "buffer_load_dwordx4 a[108:111], v3, s[44:47], s59 offen offset:0 sc1\n\t"
    "v_mul_f32 v169, v247, v169\n\t"
    "v_mul_f32 v170, v248, v170\n\t"
    "v_mul_f32 v171, v249, v171\n\t"
    "v_mul_f32 v172, v250, v172\n\t"
    "v_mul_f32 v173, v251, v173\n\t"
    "v_mul_f32 v174, v252, v174\n\t"
    "v_mfma_f32_16x16x32_bf16 v[212:215], a[92:95], a[52:55], v[212:215]\n\t"
    "v_mul_f32 v175, v253, v175\n\t"
    "v_mul_f32 v168, v184, v168\n\t"
    "v_mul_f32 v169, v185, v169\n\t"
    "v_mul_f32 v170, v186, v170\n\t"
    "buffer_load_dwordx4 a[112:115], v0, s[44:47], s59 offen offset:1024 sc1\n\t"
    "v_mul_f32 v171, v187, v171\n\t"
    "v_mfma_f32_16x16x32_bf16 v[208:211], a[92:95], a[48:51], v[208:211]\n\t"
    "v_mul_f32 v172, v188, v172\n\t"
    "v_mul_f32 v173, v189, v173\n\t"
    "v_mul_f32 v174, v190, v174\n\t"
    "v_mul_f32 v175, v191, v175\n\t"
    "v_cvt_pk_bf16_f32 v184, v168, v169\n\t"
    "v_cvt_pk_bf16_f32 v186, v172, v173\n\t"
    "v_mfma_f32_16x16x32_bf16 v[208:211], a[88:91], a[32:35], v[208:211]\n\t"
    "s_barrier\n\t"
    "v_cvt_pk_bf16_f32 v185, v170, v171\n\t"
    "v_cvt_pk_bf16_f32 v187, v174, v175\n\t"
    "buffer_load_dwordx4 a[116:119], v1, s[44:47], s59 offen offset:1024 sc1\n\t"
    "v_permlane16_swap_b32 v184, v186\n\t"
    "s_nop 0\n\t"
    "v_permlane16_swap_b32 v185, v187\n\t"
    "s_nop 7\n\t"
    "buffer_store_dwordx4 v[184:187], v245, s[44:47], s39 offen offset:1536 sc1\n\t"
    "v_pk_mul_f32 v[246:247], v[192:193], s[62:63]\n\t"
    "v_pk_mul_f32 v[248:249], v[194:195], s[62:63]\n\t"
    "v_pk_mul_f32 v[250:251], v[196:197], s[62:63]\n\t"
    "v_pk_mul_f32 v[252:253], v[198:199], s[62:63]\n\t"
    "v_exp_f32 v246, v246\n\t"
    "v_exp_f32 v247, v247\n\t"
    "buffer_load_dwordx4 a[120:123], v2, s[44:47], s59 offen offset:1024 sc1\n\t"
    "v_exp_f32 v248, v248\n\t"
    "v_exp_f32 v249, v249\n\t"
    "v_exp_f32 v250, v250\n\t"
    "v_exp_f32 v251, v251\n\t"
    "v_exp_f32 v252, v252\n\t"
    "v_exp_f32 v253, v253\n\t"
    "v_pk_add_f32 v[246:247], v[246:247], 1.0 op_sel_hi:[1,0]\n\t"
    "v_pk_add_f32 v[248:249], v[248:249], 1.0 op_sel_hi:[1,0]\n\t"
    "v_pk_add_f32 v[250:251], v[250:251], 1.0 op_sel_hi:[1,0]\n\t"
    "v_pk_add_f32 v[252:253], v[252:253], 1.0 op_sel_hi:[1,0]\n\t"
    "v_rcp_f32 v246, v246\n\t"
    "buffer_load_dwordx4 a[124:127], v3, s[44:47], s59 offen offset:1024 sc1\n\t"
    "v_rcp_f32 v247, v247\n\t"
    "v_rcp_f32 v248, v248\n\t"
    "v_rcp_f32 v249, v249\n\t"
    "v_rcp_f32 v250, v250\n\t"
    "v_rcp_f32 v251, v251\n\t"
    "v_rcp_f32 v252, v252\n\t"
    "v_rcp_f32 v253, v253\n\t"
    "v_pk_mul_f32 v[192:193], v[192:193], v[246:247]\n\t"
    "v_pk_mul_f32 v[194:195], v[194:195], v[248:249]\n\t"
    "v_pk_mul_f32 v[196:197], v[196:197], v[250:251]\n\t"
    "v_pk_mul_f32 v[198:199], v[198:199], v[252:253]\n\t"
    "buffer_load_dwordx4 a[128:131], v0, s[44:47], s59 offen offset:2048 sc1\n\t"
    "v_pk_mul_f32 v[192:193], v[192:193], v[208:209]\n\t"
    "v_pk_mul_f32 v[194:195], v[194:195], v[210:211]\n\t"
    "v_pk_mul_f32 v[196:197], v[196:197], v[212:213]\n\t"
    "v_pk_mul_f32 v[198:199], v[198:199], v[214:215]\n\t"
    "v_cvt_pk_bf16_f32 v208, v192, v193\n\t"
    "v_cvt_pk_bf16_f32 v210, v196, v197\n\t"
    "v_cvt_pk_bf16_f32 v209, v194, v195\n\t"
    "v_cvt_pk_bf16_f32 v211, v198, v199\n\t"
    "v_permlane16_swap_b32 v208, v210\n\t"
    "s_nop 0\n\t"
    "v_permlane16_swap_b32 v209, v211\n\t"
    "buffer_load_dwordx4 a[132:135], v1, s[44:47], s59 offen offset:2048 sc1\n\t"
    "buffer_store_dwordx4 v[208:211], v245, s[44:47], s40 offen offset:0 sc1\n\t"
    "v_pk_mul_f32 v[246:247], v[200:201], s[62:63]\n\t"
    "v_pk_mul_f32 v[248:249], v[202:203], s[62:63]\n\t"
    "v_pk_mul_f32 v[250:251], v[204:205], s[62:63]\n\t"
    "v_pk_mul_f32 v[252:253], v[206:207], s[62:63]\n\t"
    "v_exp_f32 v246, v246\n\t"
    "v_exp_f32 v247, v247\n\t"
    "v_exp_f32 v248, v248\n\t"
    "v_exp_f32 v249, v249\n\t"
    "v_exp_f32 v250, v250\n\t"
    "v_exp_f32 v251, v251\n\t"
    "buffer_load_dwordx4 a[136:139], v2, s[44:47], s59 offen offset:2048 sc1\n\t"
    "v_exp_f32 v252, v252\n\t"
    "v_exp_f32 v253, v253\n\t"
    "v_pk_add_f32 v[246:247], v[246:247], 1.0 op_sel_hi:[1,0]\n\t"
    "v_pk_add_f32 v[248:249], v[248:249], 1.0 op_sel_hi:[1,0]\n\t"
    "v_pk_add_f32 v[250:251], v[250:251], 1.0 op_sel_hi:[1,0]\n\t"
    "v_pk_add_f32 v[252:253], v[252:253], 1.0 op_sel_hi:[1,0]\n\t"
    "v_rcp_f32 v246, v246\n\t"
    "v_rcp_f32 v247, v247\n\t"
    "v_rcp_f32 v248, v248\n\t"
    "v_rcp_f32 v249, v249\n\t"
    "v_rcp_f32 v250, v250\n\t"
    "buffer_load_dwordx4 a[140:143], v3, s[44:47], s59 offen offset:2048 sc1\n\t"
    "v_rcp_f32 v251, v251\n\t"
    "v_rcp_f32 v252, v252\n\t"
    "v_rcp_f32 v253, v253\n\t"
    "v_pk_mul_f32 v[200:201], v[200:201], v[246:247]\n\t"
    "v_pk_mul_f32 v[202:203], v[202:203], v[248:249]\n\t"
    "v_pk_mul_f32 v[204:205], v[204:205], v[250:251]\n\t"
    "v_pk_mul_f32 v[206:207], v[206:207], v[252:253]\n\t"
    "ds_read_b64_tr_b16 a[64:65], v4 offset:0\n\t"
    "ds_read_b64_tr_b16 a[66:67], v4 offset:512\n\t"
    "ds_read_b64_tr_b16 a[68:69], v4 offset:4096\n\t"
    "ds_read_b64_tr_b16 a[70:71], v4 offset:4608\n\t"
    "ds_read_b64_tr_b16 a[72:73], v5 offset:0\n\t"
    "ds_read_b64_tr_b16 a[74:75], v5 offset:512\n\t"
    "ds_read_b64_tr_b16 a[76:77], v5 offset:4096\n\t"
    "ds_read_b64_tr_b16 a[78:79], v5 offset:4608\n\t"
    "v_pk_mul_f32 v[200:201], v[200:201], v[216:217]\n\t"
    "v_pk_mul_f32 v[202:203], v[202:203], v[218:219]\n\t"
    "v_pk_mul_f32 v[204:205], v[204:205], v[220:221]\n\t"
    "v_pk_mul_f32 v[206:207], v[206:207], v[222:223]\n\t"
    "v_cvt_pk_bf16_f32 v216, v200, v201\n\t"
    "v_cvt_pk_bf16_f32 v218, v204, v205\n\t"
    "v_cvt_pk_bf16_f32 v217, v202, v203\n\t"
    "v_cvt_pk_bf16_f32 v219, v206, v207\n\t"
    "v_permlane16_swap_b32 v216, v218\n\t"
    "s_nop 0\n\t"
    "v_permlane16_swap_b32 v217, v219\n\t"
    "buffer_store_dwordx4 v[216:219], v245, s[44:47], s41 offen offset:0 sc1\n\t"
    "s_nop 1\n\t"
    "G2S_%=:\n\t"
    "s_waitcnt vmcnt(6) lgkmcnt(4)\n\t"
    "v_mfma_f32_16x16x32_bf16 v[96:99], a[64:67], a[96:99], 0\n\t"
    "ds_read_b64_tr_b16 a[80:81], v6 offset:0\n\t"
    "v_mfma_f32_16x16x32_bf16 v[96:99], a[68:71], a[112:115], v[96:99]\n\t"
    "buffer_load_dwordx4 a[144:147], v0, s[44:47], s59 offen offset:3072 sc1\n\t"
    "v_mfma_f32_16x16x32_bf16 v[100:103], a[68:71], a[116:119], 0\n\t"
    "ds_read_b64_tr_b16 a[82:83], v6 offset:512\n\t"
    "v_add_u32 v8, 0x10000, v4\n\t"
    "v_mfma_f32_16x16x32_bf16 v[100:103], a[64:67], a[100:103], v[100:103]\n\t"
    "v_mfma_f32_16x16x32_bf16 v[104:107], a[64:67], a[104:107], 0\n\t"
    "ds_read_b64_tr_b16 a[84:85], v6 offset:4096\n\t"
    "v_add_u32 v9, 0x10000, v5\n\t"
    "v_mfma_f32_16x16x32_bf16 v[104:107], a[68:71], a[120:123], v[104:107]\n\t"
    "buffer_load_dwordx4 a[148:151], v1, s[44:47], s59 offen offset:3072 sc1\n\t"
    "v_mfma_f32_16x16x32_bf16 v[108:111], a[68:71], a[124:127], 0\n\t"
    "ds_read_b64_tr_b16 a[86:87], v6 offset:4608\n\t"
    "v_add_u32 v10, 0x10000, v6\n\t"
    "v_mfma_f32_16x16x32_bf16 v[108:111], a[64:67], a[108:111], v[108:111]\n\t"
    "s_waitcnt lgkmcnt(4)\n\t"
    "v_mfma_f32_16x16x32_bf16 v[124:127], a[72:75], a[108:111], 0\n\t"
    "ds_read_b64_tr_b16 a[88:89], v7 offset:0\n\t"
    "v_add_u32 v11, 0x10000, v7\n\t"
    "v_mfma_f32_16x16x32_bf16 v[124:127], a[76:79], a[124:127], v[124:127]\n\t"
    "buffer_load_dwordx4 a[152:155], v2, s[44:47], s59 offen offset:3072 sc1\n\t"
    "v_mfma_f32_16x16x32_bf16 v[120:123], a[76:79], a[120:123], 0\n\t"
    "ds_read_b64_tr_b16 a[90:91], v7 offset:512\n\t"
    "v_add_u32 v12, 0x20000, v4\n\t"
    "v_mfma_f32_16x16x32_bf16 v[120:123], a[72:75], a[104:107], v[120:123]\n\t"
    "v_mfma_f32_16x16x32_bf16 v[116:119], a[72:75], a[100:103], 0\n\t"
    "ds_read_b64_tr_b16 a[92:93], v7 offset:4096\n\t"
    "v_add_u32 v13, 0x20000, v5\n\t"
    "v_mfma_f32_16x16x32_bf16 v[116:119], a[76:79], a[116:119], v[116:119]\n\t"
    "buffer_load_dwordx4 a[156:159], v3, s[44:47], s59 offen offset:3072 sc1\n\t"
    "v_mfma_f32_16x16x32_bf16 v[112:115], a[76:79], a[112:115], 0\n\t"
    "ds_read_b64_tr_b16 a[94:95], v7 offset:4608\n\t"
    "v_add_u32 v14, 0x20000, v6\n\t"
    "v_mfma_f32_16x16x32_bf16 v[112:115], a[72:75], a[96:99], v[112:115]\n\t"
    "s_waitcnt lgkmcnt(4)\n\t"
    "v_mfma_f32_16x16x32_bf16 v[128:131], a[80:83], a[96:99], 0\n\t"
    "ds_read_b64_tr_b16 a[64:65], v4 offset:16384\n\t"
    "v_add_u32 v15, 0x20000, v7\n\t"
    "v_mfma_f32_16x16x32_bf16 v[128:131], a[84:87], a[112:115], v[128:131]\n\t"
    "v_mfma_f32_16x16x32_bf16 v[132:135], a[84:87], a[116:119], 0\n\t"
    "s_lshl1_add_u32 s59, s82, 4096\n\t"
    "ds_read_b64_tr_b16 a[66:67], v4 offset:16896\n\t"
    "v_accvgpr_read_b32 v16, a229\n\t"
    "v_mfma_f32_16x16x32_bf16 v[132:135], a[80:83], a[100:103], v[132:135]\n\t"
    "buffer_load_dwordx4 a[160:163], v0, s[44:47], s59 offen offset:0 sc1\n\t"
    "v_mfma_f32_16x16x32_bf16 v[136:139], a[80:83], a[104:107], 0\n\t"
    "ds_read_b64_tr_b16 a[68:69], v4 offset:20480\n\t"
    "v_accvgpr_read_b32 v17, a230\n\t"
    "v_mfma_f32_16x16x32_bf16 v[136:139], a[84:87], a[120:123], v[136:139]\n\t"
    "v_mfma_f32_16x16x32_bf16 v[140:143], a[84:87], a[124:127], 0\n\t"
    "ds_read_b64_tr_b16 a[70:71], v4 offset:20992\n\t"
    "v_accvgpr_read_b32 v18, a231\n\t"
    "v_mfma_f32_16x16x32_bf16 v[140:143], a[80:83], a[108:111], v[140:143]\n\t"
    "s_waitcnt lgkmcnt(4)\n\t"
    "v_mfma_f32_16x16x32_bf16 v[156:159], a[88:91], a[108:111], 0\n\t"
    "ds_read_b64_tr_b16 a[72:73], v5 offset:16384\n\t"
    "v_accvgpr_read_b32 v21, a232\n\t"
    "v_mfma_f32_16x16x32_bf16 v[156:159], a[92:95], a[124:127], v[156:159]\n\t"
    "buffer_load_dwordx4 a[164:167], v1, s[44:47], s59 offen offset:0 sc1\n\t"
    "v_mfma_f32_16x16x32_bf16 v[152:155], a[92:95], a[120:123], 0\n\t"
    "ds_read_b64_tr_b16 a[74:75], v5 offset:16896\n\t"
    "v_accvgpr_read_b32 v24, a227\n\t"
    "v_mfma_f32_16x16x32_bf16 v[152:155], a[88:91], a[104:107], v[152:155]\n\t"
    "buffer_load_dwordx4 a[168:171], v2, s[44:47], s59 offen offset:0 sc1\n\t"
    "v_mfma_f32_16x16x32_bf16 v[148:151], a[88:91], a[100:103], 0\n\t"
    "ds_read_b64_tr_b16 a[76:77], v5 offset:20480\n\t"
    "v_accvgpr_read_b32 v25, a234\n\t"
    "v_mfma_f32_16x16x32_bf16 v[148:151], a[92:95], a[116:119], v[148:151]\n\t"
    "v_mfma_f32_16x16x32_bf16 v[144:147], a[92:95], a[112:115], 0\n\t"
    "ds_read_b64_tr_b16 a[78:79], v5 offset:20992\n\t"
    "v_and_b32 v19, 3, v255\n\t"
    "v_mfma_f32_16x16x32_bf16 v[144:147], a[88:91], a[96:99], v[144:147]\n\t"
    "s_waitcnt lgkmcnt(4)\n\t"
    "v_mfma_f32_16x16x32_bf16 v[160:163], a[64:67], a[96:99], 0\n\t"
    "ds_read_b64_tr_b16 a[80:81], v6 offset:16384\n\t"
    "v_add_u32 v19, 1, v19\n\t"
    "v_mfma_f32_16x16x32_bf16 v[160:163], a[68:71], a[112:115], v[160:163]\n\t"
    "buffer_load_dwordx4 a[172:175], v3, s[44:47], s59 offen offset:0 sc1\n\t"
    "v_mfma_f32_16x16x32_bf16 v[164:167], a[68:71], a[116:119], 0\n\t"
    "ds_read_b64_tr_b16 a[82:83], v6 offset:16896\n\t"
    "v_min_u32 v19, 3, v19\n\t"
    "v_mfma_f32_16x16x32_bf16 v[164:167], a[64:67], a[100:103], v[164:167]\n\t"
    "buffer_load_dwordx4 a[176:179], v0, s[44:47], s59 offen offset:1024 sc1\n\t"
    "v_mfma_f32_16x16x32_bf16 v[168:171], a[64:67], a[104:107], 0\n\t"
    "ds_read_b64_tr_b16 a[84:85], v6 offset:20480\n\t"
    "v_max_u32 v20, 1, v19\n\t"
    "v_mfma_f32_16x16x32_bf16 v[168:171], a[68:71], a[120:123], v[168:171]\n\t"
    "v_mfma_f32_16x16x32_bf16 v[172:175], a[68:71], a[124:127], 0\n\t"
    "ds_read_b64_tr_b16 a[86:87], v6 offset:20992\n\t"
    "v_lshlrev_b32 v19, 10, v19\n\t"
    "v_mfma_f32_16x16x32_bf16 v[172:175], a[64:67], a[108:111], v[172:175]\n\t"
    "s_waitcnt lgkmcnt(4)\n\t"
    "v_mfma_f32_16x16x32_bf16 v[188:191], a[72:75], a[108:111], 0\n\t"
    "ds_read_b64_tr_b16 a[88:89], v7 offset:16384\n\t"
    "v_xor_b32 v19, v19, v21\n\t"
    "v_mfma_f32_16x16x32_bf16 v[188:191], a[76:79], a[124:127], v[188:191]\n\t"
    "buffer_load_dwordx4 a[180:183], v1, s[44:47], s59 offen offset:1024 sc1\n\t"
    "v_mfma_f32_16x16x32_bf16 v[184:187], a[76:79], a[120:123], 0\n\t"
    "ds_read_b64_tr_b16 a[90:91], v7 offset:16896\n\t"
    "v_lshl_add_u32 v19, v20, 7, v19\n\t"
    "v_mfma_f32_16x16x32_bf16 v[184:187], a[72:75], a[104:107], v[184:187]\n\t"
    "s_waitcnt vmcnt(10)\n\t"
    "s_barrier\n\t"
    "buffer_store_dwordx2 v[232:233], v236, s[44:47], 0 offen offset:2048 sc1\n\t"
    "v_mfma_f32_16x16x32_bf16 v[180:183], a[72:75], a[100:103], 0\n\t"
    "ds_read_b64_tr_b16 a[92:93], v7 offset:20480\n\t"
    "v_mov_b32 v22, 0\n\t"
    "v_mfma_f32_16x16x32_bf16 v[180:183], a[76:79], a[116:119], v[180:183]\n\t"
    "buffer_load_dwordx4 a[184:187], v2, s[44:47], s59 offen offset:1024 sc1\n\t"
    "v_mfma_f32_16x16x32_bf16 v[176:179], a[76:79], a[112:115], 0\n\t"
    "ds_read_b64_tr_b16 a[94:95], v7 offset:20992\n\t"
    "v_mov_b32 v23, 0\n\t"
    "v_mfma_f32_16x16x32_bf16 v[176:179], a[72:75], a[96:99], v[176:179]\n\t"
    "s_waitcnt lgkmcnt(4)\n\t"
    "v_mfma_f32_16x16x32_bf16 v[192:195], a[80:83], a[96:99], 0\n\t"
    "ds_read_b64_tr_b16 a[64:65], v4 offset:8192\n\t"
    "v_mov_b32 v26, 0\n\t"
    "v_mfma_f32_16x16x32_bf16 v[192:195], a[84:87], a[112:115], v[192:195]\n\t"
    "buffer_load_dwordx4 a[188:191], v3, s[44:47], s59 offen offset:1024 sc1\n\t"
    "v_mfma_f32_16x16x32_bf16 v[196:199], a[84:87], a[116:119], 0\n\t"
    "ds_read_b64_tr_b16 a[66:67], v4 offset:8704\n\t"
    "v_mov_b32 v27, 0\n\t"
    "v_mfma_f32_16x16x32_bf16 v[196:199], a[80:83], a[100:103], v[196:199]\n\t"
    "v_mfma_f32_16x16x32_bf16 v[200:203], a[80:83], a[104:107], 0\n\t"
    "ds_read_b64_tr_b16 a[68:69], v4 offset:12288\n\t"
    "s_mul_i32 s80, s2, 49536\n\t"
    "v_mfma_f32_16x16x32_bf16 v[200:203], a[84:87], a[120:123], v[200:203]\n\t"
    "buffer_load_dwordx4 a[192:195], v0, s[44:47], s59 offen offset:2048 sc1\n\t"
    "v_mfma_f32_16x16x32_bf16 v[204:207], a[84:87], a[124:127], 0\n\t"
    "ds_read_b64_tr_b16 a[70:71], v4 offset:12800\n\t"
    "s_add_u32 s80, s80, 16777216\n\t"
    "v_mfma_f32_16x16x32_bf16 v[204:207], a[80:83], a[108:111], v[204:207]\n\t"
    "s_waitcnt lgkmcnt(4)\n\t"
    "v_mfma_f32_16x16x32_bf16 v[220:223], a[88:91], a[108:111], 0\n\t"
    "ds_read_b64_tr_b16 a[72:73], v5 offset:8192\n\t"
    "s_xor_b32 s4, s2, 2\n\t"
    "v_mfma_f32_16x16x32_bf16 v[220:223], a[92:95], a[124:127], v[220:223]\n\t"
    "buffer_load_dwordx4 a[196:199], v1, s[44:47], s59 offen offset:2048 sc1\n\t"
    "v_mfma_f32_16x16x32_bf16 v[216:219], a[92:95], a[120:123], 0\n\t"
    "ds_read_b64_tr_b16 a[74:75], v5 offset:8704\n\t"
    "s_mul_i32 s4, s4, 49536\n\t"
    "v_mfma_f32_16x16x32_bf16 v[216:219], a[88:91], a[104:107], v[216:219]\n\t"
    "v_mfma_f32_16x16x32_bf16 v[212:215], a[88:91], a[100:103], 0\n\t"
    "ds_read_b64_tr_b16 a[76:77], v5 offset:12288\n\t"
    "s_add_u32 s4, s4, 16777216\n\t"
    "v_mfma_f32_16x16x32_bf16 v[212:215], a[92:95], a[116:119], v[212:215]\n\t"
    "buffer_load_dwordx4 a[200:203], v2, s[44:47], s59 offen offset:2048 sc1\n\t"
    "v_mfma_f32_16x16x32_bf16 v[208:211], a[92:95], a[112:115], 0\n\t"
    "ds_read_b64_tr_b16 a[78:79], v5 offset:12800\n\t"
    "s_mov_b32 s36, 32768\n\t"
    "v_mfma_f32_16x16x32_bf16 v[208:211], a[88:91], a[96:99], v[208:211]\n\t"
    "s_waitcnt vmcnt(12) lgkmcnt(4)\n\t"
    "v_mfma_f32_16x16x32_bf16 v[96:99], a[64:67], a[128:131], v[96:99]\n\t"
    "ds_read_b64_tr_b16 a[80:81], v6 offset:8192\n\t"
    "s_xor_b32 s5, s2, 4\n\t"
    "v_mfma_f32_16x16x32_bf16 v[96:99], a[68:71], a[144:147], v[96:99]\n\t"
    "buffer_load_dwordx4 a[204:207], v3, s[44:47], s59 offen offset:2048 sc1\n\t"
    "v_mfma_f32_16x16x32_bf16 v[100:103], a[68:71], a[148:151], v[100:103]\n\t"
    "ds_read_b64_tr_b16 a[82:83], v6 offset:8704\n\t"
    "s_mul_i32 s5, s5, 49536\n\t"
    "v_mfma_f32_16x16x32_bf16 v[100:103], a[64:67], a[132:135], v[100:103]\n\t"
    "v_mfma_f32_16x16x32_bf16 v[104:107], a[64:67], a[136:139], v[104:107]\n\t"
    "ds_read_b64_tr_b16 a[84:85], v6 offset:12288\n\t"
    "s_add_u32 s5, s5, 16793728\n\t"
    "v_mfma_f32_16x16x32_bf16 v[104:107], a[68:71], a[152:155], v[104:107]\n\t"
    "buffer_load_dwordx4 a[208:211], v0, s[44:47], s59 offen offset:3072 sc1\n\t"
    "v_mfma_f32_16x16x32_bf16 v[108:111], a[68:71], a[156:159], v[108:111]\n\t"
    "ds_read_b64_tr_b16 a[86:87], v6 offset:12800\n\t"
    "s_mov_b32 s37, 65536\n\t"
    "v_mfma_f32_16x16x32_bf16 v[108:111], a[64:67], a[140:143], v[108:111]\n\t"
    "s_waitcnt lgkmcnt(4)\n\t"
    "v_mfma_f32_16x16x32_bf16 v[124:127], a[72:75], a[140:143], v[124:127]\n\t"
    "ds_read_b64_tr_b16 a[88:89], v7 offset:8192\n\t"
    "s_xor_b32 s6, s2, 6\n\t"
    "v_mfma_f32_16x16x32_bf16 v[124:127], a[76:79], a[156:159], v[124:127]\n\t"
    "buffer_load_dwordx4 a[212:215], v1, s[44:47], s59 offen offset:3072 sc1\n\t"
    "v_mfma_f32_16x16x32_bf16 v[120:123], a[76:79], a[152:155], v[120:123]\n\t"
    "ds_read_b64_tr_b16 a[90:91], v7 offset:8704\n\t"
    "s_mul_i32 s6, s6, 49536\n\t"
    "v_mfma_f32_16x16x32_bf16 v[120:123], a[72:75], a[136:139], v[120:123]\n\t"
    "v_mfma_f32_16x16x32_bf16 v[116:119], a[72:75], a[132:135], v[116:119]\n\t"
    "ds_read_b64_tr_b16 a[92:93], v7 offset:12288\n\t"
    "s_add_u32 s6, s6, 16810240\n\t"
    "v_mfma_f32_16x16x32_bf16 v[116:119], a[76:79], a[148:151], v[116:119]\n\t"
    "buffer_load_dwordx4 a[216:219], v2, s[44:47], s59 offen offset:3072 sc1\n\t"
    "v_mfma_f32_16x16x32_bf16 v[112:115], a[76:79], a[144:147], v[112:115]\n\t"
    "ds_read_b64_tr_b16 a[94:95], v7 offset:12800\n\t"
    "s_mov_b32 s38, 98304\n\t"
    "v_mfma_f32_16x16x32_bf16 v[112:115], a[72:75], a[128:131], v[112:115]\n\t"
    "s_waitcnt lgkmcnt(4)\n\t"
    "v_mfma_f32_16x16x32_bf16 v[128:131], a[80:83], a[128:131], v[128:131]\n\t"
    "ds_read_b64_tr_b16 a[64:65], v4 offset:24576\n\t"
    "s_mov_b32 s7, 0\n\t"
    "v_mfma_f32_16x16x32_bf16 v[128:131], a[84:87], a[144:147], v[128:131]\n\t"
    "buffer_load_dwordx4 a[220:223], v3, s[44:47], s59 offen offset:3072 sc1\n\t"
    "v_mfma_f32_16x16x32_bf16 v[132:135], a[84:87], a[148:151], v[132:135]\n\t"
    "ds_read_b64_tr_b16 a[66:67], v4 offset:25088\n\t"
    "s_mov_b32 s39, 0\n\t"
    "v_mfma_f32_16x16x32_bf16 v[132:135], a[80:83], a[132:135], v[132:135]\n\t"
    "v_mfma_f32_16x16x32_bf16 v[136:139], a[80:83], a[136:139], v[136:139]\n\t"
    "ds_read_b64_tr_b16 a[68:69], v4 offset:28672\n\t"
    "v_mfma_f32_16x16x32_bf16 v[136:139], a[84:87], a[152:155], v[136:139]\n\t"
    "v_mfma_f32_16x16x32_bf16 v[140:143], a[84:87], a[156:159], v[140:143]\n\t"
    "ds_read_b64_tr_b16 a[70:71], v4 offset:29184\n\t"
    "v_mfma_f32_16x16x32_bf16 v[140:143], a[80:83], a[140:143], v[140:143]\n\t"
    "s_waitcnt lgkmcnt(4)\n\t"
    "v_mfma_f32_16x16x32_bf16 v[156:159], a[88:91], a[140:143], v[156:159]\n\t"
    "s_add_u32 s58, s70, 458752\n\t"
    "s_add_u32 m0, s72, 131072\n\t"
    "ds_read_b64_tr_b16 a[72:73], v5 offset:24576\n\t"
    "v_mfma_f32_16x16x32_bf16 v[156:159], a[92:95], a[156:159], v[156:159]\n\t"
    "buffer_load_dwordx4 v224, s[48:51], s58 offen offset:0 lds\n\t"
    "v_mfma_f32_16x16x32_bf16 v[152:155], a[92:95], a[152:155], v[152:155]\n\t"
    "ds_read_b64_tr_b16 a[74:75], v5 offset:25088\n\t"
    "v_mfma_f32_16x16x32_bf16 v[152:155], a[88:91], a[136:139], v[152:155]\n\t"
    "buffer_load_dwordx4 v225, s[48:51], s58 offen offset:1024 lds\n\t"
    "v_mfma_f32_16x16x32_bf16 v[148:151], a[88:91], a[132:135], v[148:151]\n\t"
    "ds_read_b64_tr_b16 a[76:77], v5 offset:28672\n\t"
    "v_mfma_f32_16x16x32_bf16 v[148:151], a[92:95], a[148:151], v[148:151]\n\t"
    "v_mfma_f32_16x16x32_bf16 v[144:147], a[92:95], a[144:147], v[144:147]\n\t"
    "ds_read_b64_tr_b16 a[78:79], v5 offset:29184\n\t"
    "v_mfma_f32_16x16x32_bf16 v[144:147], a[88:91], a[128:131], v[144:147]\n\t"
    "s_waitcnt lgkmcnt(4)\n\t"
    "v_mfma_f32_16x16x32_bf16 v[160:163], a[64:67], a[128:131], v[160:163]\n\t"
    "ds_read_b64_tr_b16 a[80:81], v6 offset:24576\n\t"
    "v_mfma_f32_16x16x32_bf16 v[160:163], a[68:71], a[144:147], v[160:163]\n\t"
    "buffer_load_dwordx4 v226, s[48:51], s58 offen offset:2048 lds\n\t"
    "v_mfma_f32_16x16x32_bf16 v[164:167], a[68:71], a[148:151], v[164:167]\n\t"
    "ds_read_b64_tr_b16 a[82:83], v6 offset:25088\n\t"
    "v_mfma_f32_16x16x32_bf16 v[164:167], a[64:67], a[132:135], v[164:167]\n\t"
    "buffer_load_dwordx4 v227, s[48:51], s58 offen offset:3072 lds\n\t"
    "v_mfma_f32_16x16x32_bf16 v[168:171], a[64:67], a[136:139], v[168:171]\n\t"
    "ds_read_b64_tr_b16 a[84:85], v6 offset:28672\n\t"
    "v_mfma_f32_16x16x32_bf16 v[168:171], a[68:71], a[152:155], v[168:171]\n\t"
    "buffer_load_dword a255, v19, s[44:47], 0 offen sc1\n\t"
    "v_mfma_f32_16x16x32_bf16 v[172:175], a[68:71], a[156:159], v[172:175]\n\t"
    "v_mov_b32 v240, 16384\n\t"
    "ds_read_b64_tr_b16 a[86:87], v6 offset:29184\n\t"
    "v_mfma_f32_16x16x32_bf16 v[172:175], a[64:67], a[140:143], v[172:175]\n\t"
    "s_waitcnt lgkmcnt(4)\n\t"
    "v_mfma_f32_16x16x32_bf16 v[188:191], a[72:75], a[140:143], v[188:191]\n\t"
    "ds_read_b64_tr_b16 a[88:89], v7 offset:24576\n\t"
    "v_mfma_f32_16x16x32_bf16 v[188:191], a[76:79], a[156:159], v[188:191]\n\t"
    "buffer_load_dword a255, v240, s[44:47], s80 offen\n\t"
    "v_mfma_f32_16x16x32_bf16 v[184:187], a[76:79], a[152:155], v[184:187]\n\t"
    "ds_read_b64_tr_b16 a[90:91], v7 offset:25088\n\t"
    "v_mfma_f32_16x16x32_bf16 v[184:187], a[72:75], a[136:139], v[184:187]\n\t"
    "buffer_load_dword a255, v240, s[44:47], s4 offen\n\t"
    "v_mfma_f32_16x16x32_bf16 v[180:183], a[72:75], a[132:135], v[180:183]\n\t"
    "ds_read_b64_tr_b16 a[92:93], v7 offset:28672\n\t"
    "v_mfma_f32_16x16x32_bf16 v[180:183], a[76:79], a[148:151], v[180:183]\n\t"
    "buffer_load_dword a255, v240, s[44:47], s5 offen\n\t"
    "v_mfma_f32_16x16x32_bf16 v[176:179], a[76:79], a[144:147], v[176:179]\n\t"
    "ds_read_b64_tr_b16 a[94:95], v7 offset:29184\n\t"
    "v_mfma_f32_16x16x32_bf16 v[176:179], a[72:75], a[128:131], v[176:179]\n\t"
    "s_waitcnt lgkmcnt(4)\n\t"
    "v_mfma_f32_16x16x32_bf16 v[192:195], a[80:83], a[128:131], v[192:195]\n\t"
    "ds_read_b64_tr_b16 a[64:65], v4 offset:32768\n\t"
    "v_mfma_f32_16x16x32_bf16 v[192:195], a[84:87], a[144:147], v[192:195]\n\t"
    "buffer_load_dword a255, v240, s[44:47], s6 offen\n\t"
    "v_mfma_f32_16x16x32_bf16 v[196:199], a[84:87], a[148:151], v[196:199]\n\t"
    "s_add_u32 m0, s72, 135168\n\t"
    "ds_read_b64_tr_b16 a[66:67], v4 offset:33280\n\t"
    "v_mfma_f32_16x16x32_bf16 v[196:199], a[80:83], a[132:135], v[196:199]\n\t"
    "buffer_load_dwordx4 v228, s[48:51], s58 offen offset:0 lds\n\t"
    "v_mfma_f32_16x16x32_bf16 v[200:203], a[80:83], a[136:139], v[200:203]\n\t"
    "ds_read_b64_tr_b16 a[68:69], v4 offset:36864\n\t"
    "v_mfma_f32_16x16x32_bf16 v[200:203], a[84:87], a[152:155], v[200:203]\n\t"
    "buffer_load_dwordx4 v229, s[48:51], s58 offen offset:1024 lds\n\t"
    "v_mfma_f32_16x16x32_bf16 v[204:207], a[84:87], a[156:159], v[204:207]\n\t"
    "ds_read_b64_tr_b16 a[70:71], v4 offset:37376\n\t"
    "v_mfma_f32_16x16x32_bf16 v[204:207], a[80:83], a[140:143], v[204:207]\n\t"
    "s_waitcnt lgkmcnt(4)\n\t"
    "v_mfma_f32_16x16x32_bf16 v[220:223], a[88:91], a[140:143], v[220:223]\n\t"
    "ds_read_b64_tr_b16 a[72:73], v5 offset:32768\n\t"
    "v_mfma_f32_16x16x32_bf16 v[220:223], a[92:95], a[156:159], v[220:223]\n\t"
    "buffer_load_dwordx4 v230, s[48:51], s58 offen offset:2048 lds\n\t"
    "v_mfma_f32_16x16x32_bf16 v[216:219], a[92:95], a[152:155], v[216:219]\n\t"
    "ds_read_b64_tr_b16 a[74:75], v5 offset:33280\n\t"
    "v_mfma_f32_16x16x32_bf16 v[216:219], a[88:91], a[136:139], v[216:219]\n\t"
    "v_mfma_f32_16x16x32_bf16 v[212:215], a[88:91], a[132:135], v[212:215]\n\t"
    "ds_read_b64_tr_b16 a[76:77], v5 offset:36864\n\t"
    "v_mfma_f32_16x16x32_bf16 v[212:215], a[92:95], a[148:151], v[212:215]\n\t"
    "buffer_load_dwordx4 v231, s[48:51], s58 offen offset:3072 lds\n\t"
    "v_mfma_f32_16x16x32_bf16 v[208:211], a[92:95], a[144:147], v[208:211]\n\t"
    "ds_read_b64_tr_b16 a[78:79], v5 offset:37376\n\t"
    "v_mfma_f32_16x16x32_bf16 v[208:211], a[88:91], a[128:131], v[208:211]\n\t"
    "s_waitcnt vmcnt(21) lgkmcnt(4)\n\t"
    "v_mfma_f32_16x16x32_bf16 v[96:99], a[64:67], a[160:163], v[96:99]\n\t"
    "s_add_u32 s58, s70, 917504\n\t"
    "s_add_u32 m0, s72, 65536\n\t"
    "ds_read_b64_tr_b16 a[80:81], v6 offset:32768\n\t"
    "v_mfma_f32_16x16x32_bf16 v[96:99], a[68:71], a[176:179], v[96:99]\n\t"
    "buffer_load_dwordx4 v224, s[48:51], s58 offen offset:0 lds\n\t"
    "v_mfma_f32_16x16x32_bf16 v[100:103], a[68:71], a[180:183], v[100:103]\n\t"
    "ds_read_b64_tr_b16 a[82:83], v6 offset:33280\n\t"
    "v_mfma_f32_16x16x32_bf16 v[100:103], a[64:67], a[164:167], v[100:103]\n\t"
    "v_mfma_f32_16x16x32_bf16 v[104:107], a[64:67], a[168:171], v[104:107]\n\t"
    "ds_read_b64_tr_b16 a[84:85], v6 offset:36864\n\t"
    "v_mfma_f32_16x16x32_bf16 v[104:107], a[68:71], a[184:187], v[104:107]\n\t"
    "buffer_load_dwordx2 v[234:235], v237, s[44:47], 0 offen offset:2048 sc1\n\t"
    "v_mfma_f32_16x16x32_bf16 v[108:111], a[68:71], a[188:191], v[108:111]\n\t"
    "ds_read_b64_tr_b16 a[86:87], v6 offset:37376\n\t"
    "v_mfma_f32_16x16x32_bf16 v[108:111], a[64:67], a[172:175], v[108:111]\n\t"
    "s_waitcnt lgkmcnt(4)\n\t"
    "s_barrier\n\t"
    "v_mfma_f32_16x16x32_bf16 v[124:127], a[72:75], a[172:175], v[124:127]\n\t"
    "ds_read_b64_tr_b16 a[88:89], v7 offset:32768\n\t"
    "v_mfma_f32_16x16x32_bf16 v[124:127], a[76:79], a[188:191], v[124:127]\n\t"
    "buffer_load_dwordx4 v225, s[48:51], s58 offen offset:1024 lds\n\t"
    "v_mfma_f32_16x16x32_bf16 v[120:123], a[76:79], a[184:187], v[120:123]\n\t"
    "ds_read_b64_tr_b16 a[90:91], v7 offset:33280\n\t"
    "v_mfma_f32_16x16x32_bf16 v[120:123], a[72:75], a[168:171], v[120:123]\n\t"
    "buffer_load_dwordx4 v226, s[48:51], s58 offen offset:2048 lds\n\t"
    "v_mfma_f32_16x16x32_bf16 v[116:119], a[72:75], a[164:167], v[116:119]\n\t"
    "ds_read_b64_tr_b16 a[92:93], v7 offset:36864\n\t"
    "v_mfma_f32_16x16x32_bf16 v[116:119], a[76:79], a[180:183], v[116:119]\n\t"
    "v_mfma_f32_16x16x32_bf16 v[112:115], a[76:79], a[176:179], v[112:115]\n\t"
    "ds_read_b64_tr_b16 a[94:95], v7 offset:37376\n\t"
    "v_mfma_f32_16x16x32_bf16 v[112:115], a[72:75], a[160:163], v[112:115]\n\t"
    "s_waitcnt lgkmcnt(4)\n\t"
    "v_mfma_f32_16x16x32_bf16 v[128:131], a[80:83], a[160:163], v[128:131]\n\t"
    "ds_read_b64_tr_b16 a[64:65], v4 offset:49152\n\t"
    "v_mfma_f32_16x16x32_bf16 v[128:131], a[84:87], a[176:179], v[128:131]\n\t"
    "buffer_load_dwordx4 v227, s[48:51], s58 offen offset:3072 lds\n\t"
    "v_mfma_f32_16x16x32_bf16 v[132:135], a[84:87], a[180:183], v[132:135]\n\t"
    "s_add_u32 m0, s72, 69632\n\t"
    "ds_read_b64_tr_b16 a[66:67], v4 offset:49664\n\t"
    "v_mfma_f32_16x16x32_bf16 v[132:135], a[80:83], a[164:167], v[132:135]\n\t"
    "buffer_load_dwordx4 v228, s[48:51], s58 offen offset:0 lds\n\t"
    "v_mfma_f32_16x16x32_bf16 v[136:139], a[80:83], a[168:171], v[136:139]\n\t"
    "ds_read_b64_tr_b16 a[68:69], v4 offset:53248\n\t"
    "v_mfma_f32_16x16x32_bf16 v[136:139], a[84:87], a[184:187], v[136:139]\n\t"
    "v_mfma_f32_16x16x32_bf16 v[140:143], a[84:87], a[188:191], v[140:143]\n\t"
    "ds_read_b64_tr_b16 a[70:71], v4 offset:53760\n\t"
    "v_mfma_f32_16x16x32_bf16 v[140:143], a[80:83], a[172:175], v[140:143]\n\t"
    "s_waitcnt lgkmcnt(4)\n\t"
    "v_mfma_f32_16x16x32_bf16 v[156:159], a[88:91], a[172:175], v[156:159]\n\t"
    "ds_read_b64_tr_b16 a[72:73], v5 offset:49152\n\t"
    "v_mfma_f32_16x16x32_bf16 v[156:159], a[92:95], a[188:191], v[156:159]\n\t"
    "buffer_load_dwordx4 v229, s[48:51], s58 offen offset:1024 lds\n\t"
    "v_mfma_f32_16x16x32_bf16 v[152:155], a[92:95], a[184:187], v[152:155]\n\t"
    "ds_read_b64_tr_b16 a[74:75], v5 offset:49664\n\t"
    "v_mfma_f32_16x16x32_bf16 v[152:155], a[88:91], a[168:171], v[152:155]\n\t"
    "buffer_load_dwordx4 v230, s[48:51], s58 offen offset:2048 lds\n\t"
    "v_mfma_f32_16x16x32_bf16 v[148:151], a[88:91], a[164:167], v[148:151]\n\t"
    "ds_read_b64_tr_b16 a[76:77], v5 offset:53248\n\t"
    "v_mfma_f32_16x16x32_bf16 v[148:151], a[92:95], a[180:183], v[148:151]\n\t"
    "v_mfma_f32_16x16x32_bf16 v[144:147], a[92:95], a[176:179], v[144:147]\n\t"
    "ds_read_b64_tr_b16 a[78:79], v5 offset:53760\n\t"
    "v_mfma_f32_16x16x32_bf16 v[144:147], a[88:91], a[160:163], v[144:147]\n\t"
    "s_waitcnt lgkmcnt(4)\n\t"
    "v_mfma_f32_16x16x32_bf16 v[160:163], a[64:67], a[160:163], v[160:163]\n\t"
    "ds_read_b64_tr_b16 a[80:81], v6 offset:49152\n\t"
    "v_mfma_f32_16x16x32_bf16 v[160:163], a[68:71], a[176:179], v[160:163]\n\t"
    "buffer_load_dwordx4 v231, s[48:51], s58 offen offset:3072 lds\n\t"
    "v_mfma_f32_16x16x32_bf16 v[164:167], a[68:71], a[180:183], v[164:167]\n\t"
    "s_add_u32 s58, s70, 1376256\n\t"
    "s_add_u32 m0, s72, 0\n\t"
    "ds_read_b64_tr_b16 a[82:83], v6 offset:49664\n\t"
    "v_mfma_f32_16x16x32_bf16 v[164:167], a[64:67], a[164:167], v[164:167]\n\t"
    "buffer_load_dwordx4 v224, s[48:51], s58 offen offset:0 lds\n\t"
    "v_mfma_f32_16x16x32_bf16 v[168:171], a[64:67], a[168:171], v[168:171]\n\t"
    "ds_read_b64_tr_b16 a[84:85], v6 offset:53248\n\t"
    "v_mfma_f32_16x16x32_bf16 v[168:171], a[68:71], a[184:187], v[168:171]\n\t"
    "s_waitcnt vmcnt(8)\n\t"
    "v_cmp_ne_u64 vcc, v[234:235], v[232:233]\n\t"
    "s_nop 0\n\t"
    "s_cmp_eq_u64 vcc, 0\n\t"
    "s_cbranch_scc1 POLL3OK_%=\n\t"
    "s_mov_b32 s83, 0x40000\n\t"
    "POLL3_%=:\n\t"
    "buffer_load_dwordx2 v[234:235], v237, s[44:47], 0 offen offset:2048 sc1\n\t"
    "s_waitcnt vmcnt(0)\n\t"
    "v_cmp_ne_u64 vcc, v[234:235], v[232:233]\n\t"
    "s_sub_u32 s83, s83, 1\n\t"
    "s_cmp_eq_u64 vcc, 0\n\t"
    "s_cbranch_scc1 POLL3OK_%=\n\t"
    "s_sleep 2\n\t"
    "s_cmp_lg_u32 s83, 0\n\t"
    "s_cbranch_scc1 POLL3_%=\n\t"
    "POLL3OK_%=:\n\t"
    "v_mfma_f32_16x16x32_bf16 v[172:175], a[68:71], a[188:191], v[172:175]\n\t"
    "ds_read_b64_tr_b16 a[86:87], v6 offset:53760\n\t"
    "v_mfma_f32_16x16x32_bf16 v[172:175], a[64:67], a[172:175], v[172:175]\n\t"
    "s_waitcnt lgkmcnt(4)\n\t"
    "v_mfma_f32_16x16x32_bf16 v[188:191], a[72:75], a[172:175], v[188:191]\n\t"
    "s_lshl2_add_u32 s59, s82, 32768\n\t"
    "ds_read_b64_tr_b16 a[88:89], v7 offset:49152\n\t"
    "v_mfma_f32_16x16x32_bf16 v[188:191], a[76:79], a[188:191], v[188:191]\n\t"
    "buffer_load_dwordx4 a[0:3], v0, s[44:47], s59 offen offset:0 sc1\n\t"
    "v_mfma_f32_16x16x32_bf16 v[184:187], a[76:79], a[184:187], v[184:187]\n\t"
    "ds_read_b64_tr_b16 a[90:91], v7 offset:49664\n\t"
    "v_mfma_f32_16x16x32_bf16 v[184:187], a[72:75], a[168:171], v[184:187]\n\t"
    "v_mfma_f32_16x16x32_bf16 v[180:183], a[72:75], a[164:167], v[180:183]\n\t"
    "ds_read_b64_tr_b16 a[92:93], v7 offset:53248\n\t"
    "v_mfma_f32_16x16x32_bf16 v[180:183], a[76:79], a[180:183], v[180:183]\n\t"
    "buffer_load_dwordx4 a[4:7], v1, s[44:47], s59 offen offset:0 sc1\n\t"
    "v_mfma_f32_16x16x32_bf16 v[176:179], a[76:79], a[176:179], v[176:179]\n\t"
    "ds_read_b64_tr_b16 a[94:95], v7 offset:53760\n\t"
    "v_mfma_f32_16x16x32_bf16 v[176:179], a[72:75], a[160:163], v[176:179]\n\t"
    "s_waitcnt lgkmcnt(4)\n\t"
    "v_mfma_f32_16x16x32_bf16 v[192:195], a[80:83], a[160:163], v[192:195]\n\t"
    "ds_read_b64_tr_b16 a[64:65], v4 offset:40960\n\t"
    "v_mfma_f32_16x16x32_bf16 v[192:195], a[84:87], a[176:179], v[192:195]\n\t"
    "buffer_load_dwordx4 a[8:11], v2, s[44:47], s59 offen offset:0 sc1\n\t"
    "v_mfma_f32_16x16x32_bf16 v[196:199], a[84:87], a[180:183], v[196:199]\n\t"
    "ds_read_b64_tr_b16 a[66:67], v4 offset:41472\n\t"
    "v_mfma_f32_16x16x32_bf16 v[196:199], a[80:83], a[164:167], v[196:199]\n\t"
    "v_mfma_f32_16x16x32_bf16 v[200:203], a[80:83], a[168:171], v[200:203]\n\t"
    "ds_read_b64_tr_b16 a[68:69], v4 offset:45056\n\t"
    "v_mfma_f32_16x16x32_bf16 v[200:203], a[84:87], a[184:187], v[200:203]\n\t"
    "buffer_load_dwordx4 a[12:15], v3, s[44:47], s59 offen offset:0 sc1\n\t"
    "v_mfma_f32_16x16x32_bf16 v[204:207], a[84:87], a[188:191], v[204:207]\n\t"
    "ds_read_b64_tr_b16 a[70:71], v4 offset:45568\n\t"
    "v_mfma_f32_16x16x32_bf16 v[204:207], a[80:83], a[172:175], v[204:207]\n\t"
    "s_waitcnt lgkmcnt(4)\n\t"
    "v_mfma_f32_16x16x32_bf16 v[220:223], a[88:91], a[172:175], v[220:223]\n\t"
    "ds_read_b64_tr_b16 a[72:73], v5 offset:40960\n\t"
    "v_mfma_f32_16x16x32_bf16 v[220:223], a[92:95], a[188:191], v[220:223]\n\t"
    "buffer_load_dwordx4 a[16:19], v0, s[44:47], s59 offen offset:1024 sc1\n\t"
    "v_mfma_f32_16x16x32_bf16 v[216:219], a[92:95], a[184:187], v[216:219]\n\t"
    "ds_read_b64_tr_b16 a[74:75], v5 offset:41472\n\t"
    "v_mfma_f32_16x16x32_bf16 v[216:219], a[88:91], a[168:171], v[216:219]\n\t"
    "v_mfma_f32_16x16x32_bf16 v[212:215], a[88:91], a[164:167], v[212:215]\n\t"
    "ds_read_b64_tr_b16 a[76:77], v5 offset:45056\n\t"
    "v_mfma_f32_16x16x32_bf16 v[212:215], a[92:95], a[180:183], v[212:215]\n\t"
    "buffer_load_dwordx4 a[20:23], v1, s[44:47], s59 offen offset:1024 sc1\n\t"
    "v_mfma_f32_16x16x32_bf16 v[208:211], a[92:95], a[176:179], v[208:211]\n\t"
    "ds_read_b64_tr_b16 a[78:79], v5 offset:45568\n\t"
    "v_mfma_f32_16x16x32_bf16 v[208:211], a[88:91], a[160:163], v[208:211]\n\t"
    "s_waitcnt lgkmcnt(4)\n\t"
    "v_mfma_f32_16x16x32_bf16 v[96:99], a[64:67], a[192:195], v[96:99]\n\t"
    "ds_read_b64_tr_b16 a[80:81], v6 offset:40960\n\t"
    "v_mfma_f32_16x16x32_bf16 v[96:99], a[68:71], a[208:211], v[96:99]\n\t"
    "buffer_load_dwordx4 a[24:27], v2, s[44:47], s59 offen offset:1024 sc1\n\t"
    "v_mfma_f32_16x16x32_bf16 v[100:103], a[68:71], a[212:215], v[100:103]\n\t"
    "ds_read_b64_tr_b16 a[82:83], v6 offset:41472\n\t"
    "v_mfma_f32_16x16x32_bf16 v[100:103], a[64:67], a[196:199], v[100:103]\n\t"
    "v_mfma_f32_16x16x32_bf16 v[104:107], a[64:67], a[200:203], v[104:107]\n\t"
    "ds_read_b64_tr_b16 a[84:85], v6 offset:45056\n\t"
    "v_mfma_f32_16x16x32_bf16 v[104:107], a[68:71], a[216:219], v[104:107]\n\t"
    "buffer_load_dwordx4 a[28:31], v3, s[44:47], s59 offen offset:1024 sc1\n\t"
    "v_mfma_f32_16x16x32_bf16 v[108:111], a[68:71], a[220:223], v[108:111]\n\t"
    "ds_read_b64_tr_b16 a[86:87], v6 offset:45568\n\t"
    "v_mfma_f32_16x16x32_bf16 v[108:111], a[64:67], a[204:207], v[108:111]\n\t"
    "s_waitcnt lgkmcnt(4)\n\t"
    "v_mfma_f32_16x16x32_bf16 v[124:127], a[72:75], a[204:207], v[124:127]\n\t"
    "ds_read_b64_tr_b16 a[88:89], v7 offset:40960\n\t"
    "v_mfma_f32_16x16x32_bf16 v[124:127], a[76:79], a[220:223], v[124:127]\n\t"
    "v_mfma_f32_16x16x32_bf16 v[120:123], a[76:79], a[216:219], v[120:123]\n\t"
    "ds_read_b64_tr_b16 a[90:91], v7 offset:41472\n\t"
    "v_mfma_f32_16x16x32_bf16 v[120:123], a[72:75], a[200:203], v[120:123]\n\t"
    "buffer_load_dwordx4 a[32:35], v0, s[44:47], s59 offen offset:2048 sc1\n\t"
    "v_mfma_f32_16x16x32_bf16 v[116:119], a[72:75], a[196:199], v[116:119]\n\t"
    "ds_read_b64_tr_b16 a[92:93], v7 offset:45056\n\t"
    "v_mfma_f32_16x16x32_bf16 v[116:119], a[76:79], a[212:215], v[116:119]\n\t"
    "v_mfma_f32_16x16x32_bf16 v[112:115], a[76:79], a[208:211], v[112:115]\n\t"
    "ds_read_b64_tr_b16 a[94:95], v7 offset:45568\n\t"
    "v_mfma_f32_16x16x32_bf16 v[112:115], a[72:75], a[192:195], v[112:115]\n\t"
    "s_waitcnt lgkmcnt(4)\n\t"
    "v_mfma_f32_16x16x32_bf16 v[128:131], a[80:83], a[192:195], v[128:131]\n\t"
    "ds_read_b64_tr_b16 a[64:65], v4 offset:57344\n\t"
    "v_mfma_f32_16x16x32_bf16 v[128:131], a[84:87], a[208:211], v[128:131]\n\t"
    "buffer_load_dwordx4 a[36:39], v1, s[44:47], s59 offen offset:2048 sc1\n\t"
    "v_mfma_f32_16x16x32_bf16 v[132:135], a[84:87], a[212:215], v[132:135]\n\t"
    "ds_read_b64_tr_b16 a[66:67], v4 offset:57856\n\t"
    "v_mfma_f32_16x16x32_bf16 v[132:135], a[80:83], a[196:199], v[132:135]\n\t"
    "buffer_load_dwordx4 a[40:43], v2, s[44:47], s59 offen offset:2048 sc1\n\t"
    "v_mfma_f32_16x16x32_bf16 v[136:139], a[80:83], a[200:203], v[136:139]\n\t"
    "ds_read_b64_tr_b16 a[68:69], v4 offset:61440\n\t"
    "v_mfma_f32_16x16x32_bf16 v[136:139], a[84:87], a[216:219], v[136:139]\n\t"
    "v_mfma_f32_16x16x32_bf16 v[140:143], a[84:87], a[220:223], v[140:143]\n\t"
    "ds_read_b64_tr_b16 a[70:71], v4 offset:61952\n\t"
    "v_mfma_f32_16x16x32_bf16 v[140:143], a[80:83], a[204:207], v[140:143]\n\t"
    "s_waitcnt lgkmcnt(4)\n\t"
    "v_mfma_f32_16x16x32_bf16 v[156:159], a[88:91], a[204:207], v[156:159]\n\t"
    "ds_read_b64_tr_b16 a[72:73], v5 offset:57344\n\t"
    "v_mfma_f32_16x16x32_bf16 v[156:159], a[92:95], a[220:223], v[156:159]\n\t"
    "buffer_load_dwordx4 a[44:47], v3, s[44:47], s59 offen offset:2048 sc1\n\t"
    "v_mfma_f32_16x16x32_bf16 v[152:155], a[92:95], a[216:219], v[152:155]\n\t"
    "ds_read_b64_tr_b16 a[74:75], v5 offset:57856\n\t"
    "v_mfma_f32_16x16x32_bf16 v[152:155], a[88:91], a[200:203], v[152:155]\n\t"
    "buffer_load_dwordx4 a[48:51], v0, s[44:47], s59 offen offset:3072 sc1\n\t"
    "v_mfma_f32_16x16x32_bf16 v[148:151], a[88:91], a[196:199], v[148:151]\n\t"
    "ds_read_b64_tr_b16 a[76:77], v5 offset:61440\n\t"
    "v_mfma_f32_16x16x32_bf16 v[148:151], a[92:95], a[212:215], v[148:151]\n\t"
    "v_mfma_f32_16x16x32_bf16 v[144:147], a[92:95], a[208:211], v[144:147]\n\t"
    "ds_read_b64_tr_b16 a[78:79], v5 offset:61952\n\t"
    "v_mfma_f32_16x16x32_bf16 v[144:147], a[88:91], a[192:195], v[144:147]\n\t"
    "s_waitcnt lgkmcnt(4)\n\t"
    "v_mfma_f32_16x16x32_bf16 v[160:163], a[64:67], a[192:195], v[160:163]\n\t"
    "ds_read_b64_tr_b16 a[80:81], v6 offset:57344\n\t"
    "v_mfma_f32_16x16x32_bf16 v[160:163], a[68:71], a[208:211], v[160:163]\n\t"
    "buffer_load_dwordx4 a[52:55], v1, s[44:47], s59 offen offset:3072 sc1\n\t"
    "v_mfma_f32_16x16x32_bf16 v[164:167], a[68:71], a[212:215], v[164:167]\n\t"
    "ds_read_b64_tr_b16 a[82:83], v6 offset:57856\n\t"
    "v_mfma_f32_16x16x32_bf16 v[164:167], a[64:67], a[196:199], v[164:167]\n\t"
    "buffer_load_dwordx4 a[56:59], v2, s[44:47], s59 offen offset:3072 sc1\n\t"
    "v_mfma_f32_16x16x32_bf16 v[168:171], a[64:67], a[200:203], v[168:171]\n\t"
    "ds_read_b64_tr_b16 a[84:85], v6 offset:61440\n\t"
    "v_mfma_f32_16x16x32_bf16 v[168:171], a[68:71], a[216:219], v[168:171]\n\t"
    "v_mfma_f32_16x16x32_bf16 v[172:175], a[68:71], a[220:223], v[172:175]\n\t"
    "ds_read_b64_tr_b16 a[86:87], v6 offset:61952\n\t"
    "v_mfma_f32_16x16x32_bf16 v[172:175], a[64:67], a[204:207], v[172:175]\n\t"
    "s_waitcnt lgkmcnt(4)\n\t"
    "v_mfma_f32_16x16x32_bf16 v[188:191], a[72:75], a[204:207], v[188:191]\n\t"
    "ds_read_b64_tr_b16 a[88:89], v7 offset:57344\n\t"
    "v_mfma_f32_16x16x32_bf16 v[188:191], a[76:79], a[220:223], v[188:191]\n\t"
    "buffer_load_dwordx4 a[60:63], v3, s[44:47], s59 offen offset:3072 sc1\n\t"
    "v_mfma_f32_16x16x32_bf16 v[184:187], a[76:79], a[216:219], v[184:187]\n\t"
    "ds_read_b64_tr_b16 a[90:91], v7 offset:57856\n\t"
    "v_mfma_f32_16x16x32_bf16 v[184:187], a[72:75], a[200:203], v[184:187]\n\t"
    "v_mfma_f32_16x16x32_bf16 v[180:183], a[72:75], a[196:199], v[180:183]\n\t"
    "s_lshl2_add_u32 s59, s82, 36864\n\t"
    "ds_read_b64_tr_b16 a[92:93], v7 offset:61440\n\t"
    "v_mfma_f32_16x16x32_bf16 v[180:183], a[76:79], a[212:215], v[180:183]\n\t"
    "buffer_load_dwordx4 v[32:35], v0, s[44:47], s59 offen offset:0 sc1\n\t"
    "v_mfma_f32_16x16x32_bf16 v[176:179], a[76:79], a[208:211], v[176:179]\n\t"
    "ds_read_b64_tr_b16 a[94:95], v7 offset:61952\n\t"
    "v_mfma_f32_16x16x32_bf16 v[176:179], a[72:75], a[192:195], v[176:179]\n\t"
    "s_waitcnt lgkmcnt(4)\n\t"
    "v_mfma_f32_16x16x32_bf16 v[192:195], a[80:83], a[192:195], v[192:195]\n\t"
    "ds_read_b64_tr_b16 a[64:65], v8 offset:32768\n\t"
    "v_mfma_f32_16x16x32_bf16 v[192:195], a[84:87], a[208:211], v[192:195]\n\t"
    "buffer_load_dwordx4 v[36:39], v1, s[44:47], s59 offen offset:0 sc1\n\t"
    "v_mfma_f32_16x16x32_bf16 v[196:199], a[84:87], a[212:215], v[196:199]\n\t"
    "ds_read_b64_tr_b16 a[66:67], v8 offset:33280\n\t"
    "v_mfma_f32_16x16x32_bf16 v[196:199], a[80:83], a[196:199], v[196:199]\n\t"
    "v_mfma_f32_16x16x32_bf16 v[200:203], a[80:83], a[200:203], v[200:203]\n\t"
    "ds_read_b64_tr_b16 a[68:69], v8 offset:36864\n\t"
    "v_mfma_f32_16x16x32_bf16 v[200:203], a[84:87], a[216:219], v[200:203]\n\t"
    "buffer_load_dwordx4 v[40:43], v2, s[44:47], s59 offen offset:0 sc1\n\t"
    "v_mfma_f32_16x16x32_bf16 v[204:207], a[84:87], a[220:223], v[204:207]\n\t"
    "ds_read_b64_tr_b16 a[70:71], v8 offset:37376\n\t"
    "v_mfma_f32_16x16x32_bf16 v[204:207], a[80:83], a[204:207], v[204:207]\n\t"
    "s_waitcnt lgkmcnt(4)\n\t"
    "v_mfma_f32_16x16x32_bf16 v[220:223], a[88:91], a[204:207], v[220:223]\n\t"
    "ds_read_b64_tr_b16 a[72:73], v9 offset:32768\n\t"
    "v_mfma_f32_16x16x32_bf16 v[220:223], a[92:95], a[220:223], v[220:223]\n\t"
    "buffer_load_dwordx4 v[44:47], v3, s[44:47], s59 offen offset:0 sc1\n\t"
    "v_mfma_f32_16x16x32_bf16 v[216:219], a[92:95], a[216:219], v[216:219]\n\t"
    "ds_read_b64_tr_b16 a[74:75], v9 offset:33280\n\t"
    "v_mfma_f32_16x16x32_bf16 v[216:219], a[88:91], a[200:203], v[216:219]\n\t"
    "v_mfma_f32_16x16x32_bf16 v[212:215], a[88:91], a[196:199], v[212:215]\n\t"
    "ds_read_b64_tr_b16 a[76:77], v9 offset:36864\n\t"
    "v_mfma_f32_16x16x32_bf16 v[212:215], a[92:95], a[212:215], v[212:215]\n\t"
    "buffer_load_dwordx4 v[48:51], v0, s[44:47], s59 offen offset:1024 sc1\n\t"
    "v_mfma_f32_16x16x32_bf16 v[208:211], a[92:95], a[208:211], v[208:211]\n\t"
    "ds_read_b64_tr_b16 a[78:79], v9 offset:37376\n\t"
    "v_mfma_f32_16x16x32_bf16 v[208:211], a[88:91], a[192:195], v[208:211]\n\t"
    "s_waitcnt vmcnt(13) lgkmcnt(4)\n\t"
    "v_mfma_f32_16x16x32_bf16 v[96:99], a[64:67], a[0:3], v[96:99]\n\t"
    "ds_read_b64_tr_b16 a[80:81], v10 offset:32768\n\t"
    "v_mfma_f32_16x16x32_bf16 v[96:99], a[68:71], a[16:19], v[96:99]\n\t"
    "buffer_load_dwordx4 v[52:55], v1, s[44:47], s59 offen offset:1024 sc1\n\t"
    "v_mfma_f32_16x16x32_bf16 v[100:103], a[68:71], a[20:23], v[100:103]\n\t"
    "ds_read_b64_tr_b16 a[82:83], v10 offset:33280\n\t"
    "v_mfma_f32_16x16x32_bf16 v[100:103], a[64:67], a[4:7], v[100:103]\n\t"
    "v_mfma_f32_16x16x32_bf16 v[104:107], a[64:67], a[8:11], v[104:107]\n\t"
    "ds_read_b64_tr_b16 a[84:85], v10 offset:36864\n\t"
    "v_mfma_f32_16x16x32_bf16 v[104:107], a[68:71], a[24:27], v[104:107]\n\t"
    "buffer_load_dwordx4 v[56:59], v2, s[44:47], s59 offen offset:1024 sc1\n\t"
    "v_mfma_f32_16x16x32_bf16 v[108:111], a[68:71], a[28:31], v[108:111]\n\t"
    "ds_read_b64_tr_b16 a[86:87], v10 offset:37376\n\t"
    "v_mfma_f32_16x16x32_bf16 v[108:111], a[64:67], a[12:15], v[108:111]\n\t"
    "s_waitcnt lgkmcnt(4)\n\t"
    "s_barrier\n\t"
    "v_mfma_f32_16x16x32_bf16 v[124:127], a[72:75], a[12:15], v[124:127]\n\t"
    "ds_read_b64_tr_b16 a[88:89], v11 offset:32768\n\t"
    "v_mfma_f32_16x16x32_bf16 v[124:127], a[76:79], a[28:31], v[124:127]\n\t"
    "buffer_load_dwordx4 v[60:63], v3, s[44:47], s59 offen offset:1024 sc1\n\t"
    "v_mfma_f32_16x16x32_bf16 v[120:123], a[76:79], a[24:27], v[120:123]\n\t"
    "ds_read_b64_tr_b16 a[90:91], v11 offset:33280\n\t"
    "v_mfma_f32_16x16x32_bf16 v[120:123], a[72:75], a[8:11], v[120:123]\n\t"
    "v_mfma_f32_16x16x32_bf16 v[116:119], a[72:75], a[4:7], v[116:119]\n\t"
    "ds_read_b64_tr_b16 a[92:93], v11 offset:36864\n\t"
    "v_mfma_f32_16x16x32_bf16 v[116:119], a[76:79], a[20:23], v[116:119]\n\t"
    "v_mfma_f32_16x16x32_bf16 v[112:115], a[76:79], a[16:19], v[112:115]\n\t"
    "ds_read_b64_tr_b16 a[94:95], v11 offset:37376\n\t"
    "v_mfma_f32_16x16x32_bf16 v[112:115], a[72:75], a[0:3], v[112:115]\n\t"
    "s_waitcnt lgkmcnt(4)\n\t"
    "v_mfma_f32_16x16x32_bf16 v[128:131], a[80:83], a[0:3], v[128:131]\n\t"
    "ds_read_b64_tr_b16 a[64:65], v8 offset:49152\n\t"
    "v_mfma_f32_16x16x32_bf16 v[128:131], a[84:87], a[16:19], v[128:131]\n\t"
    "buffer_load_dwordx4 v[64:67], v0, s[44:47], s59 offen offset:2048 sc1\n\t"
    "v_mfma_f32_16x16x32_bf16 v[132:135], a[84:87], a[20:23], v[132:135]\n\t"
    "ds_read_b64_tr_b16 a[66:67], v8 offset:49664\n\t"
    "v_mfma_f32_16x16x32_bf16 v[132:135], a[80:83], a[4:7], v[132:135]\n\t"
    "buffer_load_dwordx4 v[68:71], v1, s[44:47], s59 offen offset:2048 sc1\n\t"
    "v_mfma_f32_16x16x32_bf16 v[136:139], a[80:83], a[8:11], v[136:139]\n\t"
    "ds_read_b64_tr_b16 a[68:69], v8 offset:53248\n\t"
    "v_mfma_f32_16x16x32_bf16 v[136:139], a[84:87], a[24:27], v[136:139]\n\t"
    "v_mfma_f32_16x16x32_bf16 v[140:143], a[84:87], a[28:31], v[140:143]\n\t"
    "ds_read_b64_tr_b16 a[70:71], v8 offset:53760\n\t"
    "v_mfma_f32_16x16x32_bf16 v[140:143], a[80:83], a[12:15], v[140:143]\n\t"
    "s_waitcnt lgkmcnt(4)\n\t"
    "v_mfma_f32_16x16x32_bf16 v[156:159], a[88:91], a[12:15], v[156:159]\n\t"
    "ds_read_b64_tr_b16 a[72:73], v9 offset:49152\n\t"
    "v_mfma_f32_16x16x32_bf16 v[156:159], a[92:95], a[28:31], v[156:159]\n\t"
    "buffer_load_dwordx4 v[72:75], v2, s[44:47], s59 offen offset:2048 sc1\n\t"
    "v_mfma_f32_16x16x32_bf16 v[152:155], a[92:95], a[24:27], v[152:155]\n\t"
    "ds_read_b64_tr_b16 a[74:75], v9 offset:49664\n\t"
    "v_mfma_f32_16x16x32_bf16 v[152:155], a[88:91], a[8:11], v[152:155]\n\t"
    "buffer_load_dwordx4 v[76:79], v3, s[44:47], s59 offen offset:2048 sc1\n\t"
    "v_mfma_f32_16x16x32_bf16 v[148:151], a[88:91], a[4:7], v[148:151]\n\t"
    "ds_read_b64_tr_b16 a[76:77], v9 offset:53248\n\t"
    "v_mfma_f32_16x16x32_bf16 v[148:151], a[92:95], a[20:23], v[148:151]\n\t"
    "v_mfma_f32_16x16x32_bf16 v[144:147], a[92:95], a[16:19], v[144:147]\n\t"
    "ds_read_b64_tr_b16 a[78:79], v9 offset:53760\n\t"
    "v_mfma_f32_16x16x32_bf16 v[144:147], a[88:91], a[0:3], v[144:147]\n\t"
    "s_waitcnt lgkmcnt(4)\n\t"
    "v_mfma_f32_16x16x32_bf16 v[160:163], a[64:67], a[0:3], v[160:163]\n\t"
    "ds_read_b64_tr_b16 a[80:81], v10 offset:49152\n\t"
    "v_mfma_f32_16x16x32_bf16 v[160:163], a[68:71], a[16:19], v[160:163]\n\t"
    "buffer_load_dwordx4 v[80:83], v0, s[44:47], s59 offen offset:3072 sc1\n\t"
    "v_mfma_f32_16x16x32_bf16 v[164:167], a[68:71], a[20:23], v[164:167]\n\t"
    "ds_read_b64_tr_b16 a[82:83], v10 offset:49664\n\t"
    "v_mfma_f32_16x16x32_bf16 v[164:167], a[64:67], a[4:7], v[164:167]\n\t"
    "buffer_load_dwordx4 v[84:87], v1, s[44:47], s59 offen offset:3072 sc1\n\t"
    "v_mfma_f32_16x16x32_bf16 v[168:171], a[64:67], a[8:11], v[168:171]\n\t"
    "ds_read_b64_tr_b16 a[84:85], v10 offset:53248\n\t"
    "v_mfma_f32_16x16x32_bf16 v[168:171], a[68:71], a[24:27], v[168:171]\n\t"
    "v_mfma_f32_16x16x32_bf16 v[172:175], a[68:71], a[28:31], v[172:175]\n\t"
    "ds_read_b64_tr_b16 a[86:87], v10 offset:53760\n\t"
    "v_mfma_f32_16x16x32_bf16 v[172:175], a[64:67], a[12:15], v[172:175]\n\t"
    "s_waitcnt lgkmcnt(4)\n\t"
    "v_mfma_f32_16x16x32_bf16 v[188:191], a[72:75], a[12:15], v[188:191]\n\t"
    "ds_read_b64_tr_b16 a[88:89], v11 offset:49152\n\t"
    "v_mfma_f32_16x16x32_bf16 v[188:191], a[76:79], a[28:31], v[188:191]\n\t"
    "buffer_load_dwordx4 v[88:91], v2, s[44:47], s59 offen offset:3072 sc1\n\t"
    "v_mfma_f32_16x16x32_bf16 v[184:187], a[76:79], a[24:27], v[184:187]\n\t"
    "ds_read_b64_tr_b16 a[90:91], v11 offset:49664\n\t"
    "v_mfma_f32_16x16x32_bf16 v[184:187], a[72:75], a[8:11], v[184:187]\n\t"
    "buffer_load_dwordx4 v[92:95], v3, s[44:47], s59 offen offset:3072 sc1\n\t"
    "v_mfma_f32_16x16x32_bf16 v[180:183], a[72:75], a[4:7], v[180:183]\n\t"
    "ds_read_b64_tr_b16 a[92:93], v11 offset:53248\n\t"
    "v_mfma_f32_16x16x32_bf16 v[180:183], a[76:79], a[20:23], v[180:183]\n\t"
    "v_mfma_f32_16x16x32_bf16 v[176:179], a[76:79], a[16:19], v[176:179]\n\t"
    "ds_read_b64_tr_b16 a[94:95], v11 offset:53760\n\t"
    "v_mfma_f32_16x16x32_bf16 v[176:179], a[72:75], a[0:3], v[176:179]\n\t"
    "s_waitcnt lgkmcnt(4)\n\t"
    "v_mfma_f32_16x16x32_bf16 v[192:195], a[80:83], a[0:3], v[192:195]\n\t"
    "s_lshl2_add_u32 s59, s82, 40960\n\t"
    "ds_read_b64_tr_b16 a[64:65], v8 offset:40960\n\t"
    "v_mfma_f32_16x16x32_bf16 v[192:195], a[84:87], a[16:19], v[192:195]\n\t"
    "buffer_load_dwordx4 a[96:99], v0, s[44:47], s59 offen offset:0 sc1\n\t"
    "v_mfma_f32_16x16x32_bf16 v[196:199], a[84:87], a[20:23], v[196:199]\n\t"
    "ds_read_b64_tr_b16 a[66:67], v8 offset:41472\n\t"
    "v_mfma_f32_16x16x32_bf16 v[196:199], a[80:83], a[4:7], v[196:199]\n\t"
    "v_mfma_f32_16x16x32_bf16 v[200:203], a[80:83], a[8:11], v[200:203]\n\t"
    "ds_read_b64_tr_b16 a[68:69], v8 offset:45056\n\t"
    "v_mfma_f32_16x16x32_bf16 v[200:203], a[84:87], a[24:27], v[200:203]\n\t"
    "buffer_load_dwordx4 a[100:103], v1, s[44:47], s59 offen offset:0 sc1\n\t"
    "v_mfma_f32_16x16x32_bf16 v[204:207], a[84:87], a[28:31], v[204:207]\n\t"
    "ds_read_b64_tr_b16 a[70:71], v8 offset:45568\n\t"
    "v_mfma_f32_16x16x32_bf16 v[204:207], a[80:83], a[12:15], v[204:207]\n\t"
    "s_waitcnt lgkmcnt(4)\n\t"
    "v_mfma_f32_16x16x32_bf16 v[220:223], a[88:91], a[12:15], v[220:223]\n\t"
    "ds_read_b64_tr_b16 a[72:73], v9 offset:40960\n\t"
    "v_mfma_f32_16x16x32_bf16 v[220:223], a[92:95], a[28:31], v[220:223]\n\t"
    "buffer_load_dwordx4 a[104:107], v2, s[44:47], s59 offen offset:0 sc1\n\t"
    "v_mfma_f32_16x16x32_bf16 v[216:219], a[92:95], a[24:27], v[216:219]\n\t"
    "ds_read_b64_tr_b16 a[74:75], v9 offset:41472\n\t"
    "v_mfma_f32_16x16x32_bf16 v[216:219], a[88:91], a[8:11], v[216:219]\n\t"
    "v_mfma_f32_16x16x32_bf16 v[212:215], a[88:91], a[4:7], v[212:215]\n\t"
    "ds_read_b64_tr_b16 a[76:77], v9 offset:45056\n\t"
    "v_mfma_f32_16x16x32_bf16 v[212:215], a[92:95], a[20:23], v[212:215]\n\t"
    "buffer_load_dwordx4 a[108:111], v3, s[44:47], s59 offen offset:0 sc1\n\t"
    "v_mfma_f32_16x16x32_bf16 v[208:211], a[92:95], a[16:19], v[208:211]\n\t"
    "ds_read_b64_tr_b16 a[78:79], v9 offset:45568\n\t"
    "v_mfma_f32_16x16x32_bf16 v[208:211], a[88:91], a[0:3], v[208:211]\n\t"
    "s_waitcnt vmcnt(20) lgkmcnt(4)\n\t"
    "v_mfma_f32_16x16x32_bf16 v[96:99], a[64:67], a[32:35], v[96:99]\n\t"
    "ds_read_b64_tr_b16 a[80:81], v10 offset:40960\n\t"
    "v_mfma_f32_16x16x32_bf16 v[96:99], a[68:71], a[48:51], v[96:99]\n\t"
    "buffer_load_dwordx4 a[112:115], v0, s[44:47], s59 offen offset:1024 sc1\n\t"
    "v_mfma_f32_16x16x32_bf16 v[100:103], a[68:71], a[52:55], v[100:103]\n\t"
    "ds_read_b64_tr_b16 a[82:83], v10 offset:41472\n\t"
    "v_mfma_f32_16x16x32_bf16 v[100:103], a[64:67], a[36:39], v[100:103]\n\t"
    "v_mfma_f32_16x16x32_bf16 v[104:107], a[64:67], a[40:43], v[104:107]\n\t"
    "ds_read_b64_tr_b16 a[84:85], v10 offset:45056\n\t"
    "v_mfma_f32_16x16x32_bf16 v[104:107], a[68:71], a[56:59], v[104:107]\n\t"
    "buffer_load_dwordx4 a[116:119], v1, s[44:47], s59 offen offset:1024 sc1\n\t"
    "v_mfma_f32_16x16x32_bf16 v[108:111], a[68:71], a[60:63], v[108:111]\n\t"
    "ds_read_b64_tr_b16 a[86:87], v10 offset:45568\n\t"
    "v_mfma_f32_16x16x32_bf16 v[108:111], a[64:67], a[44:47], v[108:111]\n\t"
    "s_waitcnt lgkmcnt(4)\n\t"
    "v_mfma_f32_16x16x32_bf16 v[124:127], a[72:75], a[44:47], v[124:127]\n\t"
    "ds_read_b64_tr_b16 a[88:89], v11 offset:40960\n\t"
    "v_mfma_f32_16x16x32_bf16 v[124:127], a[76:79], a[60:63], v[124:127]\n\t"
    "buffer_load_dwordx4 a[120:123], v2, s[44:47], s59 offen offset:1024 sc1\n\t"
    "v_mfma_f32_16x16x32_bf16 v[120:123], a[76:79], a[56:59], v[120:123]\n\t"
    "ds_read_b64_tr_b16 a[90:91], v11 offset:41472\n\t"
    "v_mfma_f32_16x16x32_bf16 v[120:123], a[72:75], a[40:43], v[120:123]\n\t"
    "v_mfma_f32_16x16x32_bf16 v[116:119], a[72:75], a[36:39], v[116:119]\n\t"
    "ds_read_b64_tr_b16 a[92:93], v11 offset:45056\n\t"
    "v_mfma_f32_16x16x32_bf16 v[116:119], a[76:79], a[52:55], v[116:119]\n\t"
    "buffer_load_dwordx4 a[124:127], v3, s[44:47], s59 offen offset:1024 sc1\n\t"
    "v_mfma_f32_16x16x32_bf16 v[112:115], a[76:79], a[48:51], v[112:115]\n\t"
    "ds_read_b64_tr_b16 a[94:95], v11 offset:45568\n\t"
    "v_mfma_f32_16x16x32_bf16 v[112:115], a[72:75], a[32:35], v[112:115]\n\t"
    "s_waitcnt lgkmcnt(4)\n\t"
    "v_mfma_f32_16x16x32_bf16 v[128:131], a[80:83], a[32:35], v[128:131]\n\t"
    "ds_read_b64_tr_b16 a[64:65], v8 offset:57344\n\t"
    "v_mfma_f32_16x16x32_bf16 v[128:131], a[84:87], a[48:51], v[128:131]\n\t"
    "v_mfma_f32_16x16x32_bf16 v[132:135], a[84:87], a[52:55], v[132:135]\n\t"
    "ds_read_b64_tr_b16 a[66:67], v8 offset:57856\n\t"
    "v_mfma_f32_16x16x32_bf16 v[132:135], a[80:83], a[36:39], v[132:135]\n\t"
    "buffer_load_dwordx4 a[128:131], v0, s[44:47], s59 offen offset:2048 sc1\n\t"
    "v_mfma_f32_16x16x32_bf16 v[136:139], a[80:83], a[40:43], v[136:139]\n\t"
    "ds_read_b64_tr_b16 a[68:69], v8 offset:61440\n\t"
    "v_mfma_f32_16x16x32_bf16 v[136:139], a[84:87], a[56:59], v[136:139]\n\t"
    "v_mfma_f32_16x16x32_bf16 v[140:143], a[84:87], a[60:63], v[140:143]\n\t"
    "ds_read_b64_tr_b16 a[70:71], v8 offset:61952\n\t"
    "v_mfma_f32_16x16x32_bf16 v[140:143], a[80:83], a[44:47], v[140:143]\n\t"
    "s_waitcnt lgkmcnt(4)\n\t"
    "v_mfma_f32_16x16x32_bf16 v[156:159], a[88:91], a[44:47], v[156:159]\n\t"
    "ds_read_b64_tr_b16 a[72:73], v9 offset:57344\n\t"
    "v_mfma_f32_16x16x32_bf16 v[156:159], a[92:95], a[60:63], v[156:159]\n\t"
    "buffer_load_dwordx4 a[132:135], v1, s[44:47], s59 offen offset:2048 sc1\n\t"
    "v_mfma_f32_16x16x32_bf16 v[152:155], a[92:95], a[56:59], v[152:155]\n\t"
    "ds_read_b64_tr_b16 a[74:75], v9 offset:57856\n\t"
    "v_mfma_f32_16x16x32_bf16 v[152:155], a[88:91], a[40:43], v[152:155]\n\t"
    "buffer_load_dwordx4 a[136:139], v2, s[44:47], s59 offen offset:2048 sc1\n\t"
    "v_mfma_f32_16x16x32_bf16 v[148:151], a[88:91], a[36:39], v[148:151]\n\t"
    "ds_read_b64_tr_b16 a[76:77], v9 offset:61440\n\t"
    "v_mfma_f32_16x16x32_bf16 v[148:151], a[92:95], a[52:55], v[148:151]\n\t"
    "v_mfma_f32_16x16x32_bf16 v[144:147], a[92:95], a[48:51], v[144:147]\n\t"
    "ds_read_b64_tr_b16 a[78:79], v9 offset:61952\n\t"
    "v_mfma_f32_16x16x32_bf16 v[144:147], a[88:91], a[32:35], v[144:147]\n\t"
    "s_waitcnt lgkmcnt(4)\n\t"
    "v_mfma_f32_16x16x32_bf16 v[160:163], a[64:67], a[32:35], v[160:163]\n\t"
    "ds_read_b64_tr_b16 a[80:81], v10 offset:57344\n\t"
    "v_mfma_f32_16x16x32_bf16 v[160:163], a[68:71], a[48:51], v[160:163]\n\t"
    "buffer_load_dwordx4 a[140:143], v3, s[44:47], s59 offen offset:2048 sc1\n\t"
    "v_mfma_f32_16x16x32_bf16 v[164:167], a[68:71], a[52:55], v[164:167]\n\t"
    "ds_read_b64_tr_b16 a[82:83], v10 offset:57856\n\t"
    "v_mfma_f32_16x16x32_bf16 v[164:167], a[64:67], a[36:39], v[164:167]\n\t"
    "buffer_load_dwordx4 a[144:147], v0, s[44:47], s59 offen offset:3072 sc1\n\t"
    "v_mfma_f32_16x16x32_bf16 v[168:171], a[64:67], a[40:43], v[168:171]\n\t"
    "ds_read_b64_tr_b16 a[84:85], v10 offset:61440\n\t"
    "v_mfma_f32_16x16x32_bf16 v[168:171], a[68:71], a[56:59], v[168:171]\n\t"
    "v_mfma_f32_16x16x32_bf16 v[172:175], a[68:71], a[60:63], v[172:175]\n\t"
    "ds_read_b64_tr_b16 a[86:87], v10 offset:61952\n\t"
    "v_mfma_f32_16x16x32_bf16 v[172:175], a[64:67], a[44:47], v[172:175]\n\t"
    "s_waitcnt lgkmcnt(4)\n\t"
    "v_mfma_f32_16x16x32_bf16 v[188:191], a[72:75], a[44:47], v[188:191]\n\t"
    "ds_read_b64_tr_b16 a[88:89], v11 offset:57344\n\t"
    "v_mfma_f32_16x16x32_bf16 v[188:191], a[76:79], a[60:63], v[188:191]\n\t"
    "buffer_load_dwordx4 a[148:151], v1, s[44:47], s59 offen offset:3072 sc1\n\t"
    "v_mfma_f32_16x16x32_bf16 v[184:187], a[76:79], a[56:59], v[184:187]\n\t"
    "ds_read_b64_tr_b16 a[90:91], v11 offset:57856\n\t"
    "v_mfma_f32_16x16x32_bf16 v[184:187], a[72:75], a[40:43], v[184:187]\n\t"
    "buffer_load_dwordx4 a[152:155], v2, s[44:47], s59 offen offset:3072 sc1\n\t"
    "v_mfma_f32_16x16x32_bf16 v[180:183], a[72:75], a[36:39], v[180:183]\n\t"
    "ds_read_b64_tr_b16 a[92:93], v11 offset:61440\n\t"
    "v_mfma_f32_16x16x32_bf16 v[180:183], a[76:79], a[52:55], v[180:183]\n\t"
    "v_mfma_f32_16x16x32_bf16 v[176:179], a[76:79], a[48:51], v[176:179]\n\t"
    "ds_read_b64_tr_b16 a[94:95], v11 offset:61952\n\t"
    "v_mfma_f32_16x16x32_bf16 v[176:179], a[72:75], a[32:35], v[176:179]\n\t"
    "s_waitcnt lgkmcnt(4)\n\t"
    "v_mfma_f32_16x16x32_bf16 v[192:195], a[80:83], a[32:35], v[192:195]\n\t"
    "ds_read_b64_tr_b16 a[64:65], v14 offset:0\n\t"
    "v_mfma_f32_16x16x32_bf16 v[192:195], a[84:87], a[48:51], v[192:195]\n\t"
    "buffer_load_dwordx4 a[156:159], v3, s[44:47], s59 offen offset:3072 sc1\n\t"
    "v_mfma_f32_16x16x32_bf16 v[196:199], a[84:87], a[52:55], v[196:199]\n\t"
    "ds_read_b64_tr_b16 a[66:67], v14 offset:512\n\t"
    "v_mfma_f32_16x16x32_bf16 v[196:199], a[80:83], a[36:39], v[196:199]\n\t"
    "v_mfma_f32_16x16x32_bf16 v[200:203], a[80:83], a[40:43], v[200:203]\n\t"
    "ds_read_b64_tr_b16 a[68:69], v14 offset:4096\n\t"
    "v_mfma_f32_16x16x32_bf16 v[200:203], a[84:87], a[56:59], v[200:203]\n\t"
    "buffer_load_dwordx4 v225, s[48:51], s58 offen offset:1024 lds\n\t"
    "v_mfma_f32_16x16x32_bf16 v[204:207], a[84:87], a[60:63], v[204:207]\n\t"
    "ds_read_b64_tr_b16 a[70:71], v14 offset:4608\n\t"
    "v_mfma_f32_16x16x32_bf16 v[204:207], a[80:83], a[44:47], v[204:207]\n\t"
    "s_waitcnt lgkmcnt(4)\n\t"
    "v_mfma_f32_16x16x32_bf16 v[220:223], a[88:91], a[44:47], v[220:223]\n\t"
    "ds_read_b64_tr_b16 a[72:73], v15 offset:0\n\t"
    "v_mfma_f32_16x16x32_bf16 v[220:223], a[92:95], a[60:63], v[220:223]\n\t"
    "buffer_load_dwordx4 v226, s[48:51], s58 offen offset:2048 lds\n\t"
    "v_mfma_f32_16x16x32_bf16 v[216:219], a[92:95], a[56:59], v[216:219]\n\t"
    "ds_read_b64_tr_b16 a[74:75], v15 offset:512\n\t"
    "v_mfma_f32_16x16x32_bf16 v[216:219], a[88:91], a[40:43], v[216:219]\n\t"
    "v_mfma_f32_16x16x32_bf16 v[212:215], a[88:91], a[36:39], v[212:215]\n\t"
    "ds_read_b64_tr_b16 a[76:77], v15 offset:4096\n\t"
    "v_mfma_f32_16x16x32_bf16 v[212:215], a[92:95], a[52:55], v[212:215]\n\t"
    "buffer_load_dwordx4 v227, s[48:51], s58 offen offset:3072 lds\n\t"
    "v_mfma_f32_16x16x32_bf16 v[208:211], a[92:95], a[48:51], v[208:211]\n\t"
    "ds_read_b64_tr_b16 a[78:79], v15 offset:4608\n\t"
    "v_mfma_f32_16x16x32_bf16 v[208:211], a[88:91], a[32:35], v[208:211]\n\t"
    "s_waitcnt vmcnt(27) lgkmcnt(4)\n\t"
    "v_mfma_f32_16x16x32_bf16 v[128:131], a[64:67], v[32:35], v[128:131]\n\t"
    "s_add_u32 m0, s72, 4096\n\t"
    "ds_read_b64_tr_b16 a[80:81], v12 offset:16384\n\t"
    "v_mfma_f32_16x16x32_bf16 v[128:131], a[68:71], v[48:51], v[128:131]\n\t"
    "buffer_load_dwordx4 v228, s[48:51], s58 offen offset:0 lds\n\t"
    "v_mfma_f32_16x16x32_bf16 v[132:135], a[68:71], v[52:55], v[132:135]\n\t"
    "ds_read_b64_tr_b16 a[82:83], v12 offset:16896\n\t"
    "v_mfma_f32_16x16x32_bf16 v[132:135], a[64:67], v[36:39], v[132:135]\n\t"
    "v_mfma_f32_16x16x32_bf16 v[136:139], a[64:67], v[40:43], v[136:139]\n\t"
    "ds_read_b64_tr_b16 a[84:85], v12 offset:20480\n\t"
    "v_mfma_f32_16x16x32_bf16 v[136:139], a[68:71], v[56:59], v[136:139]\n\t"
    "buffer_load_dwordx4 v229, s[48:51], s58 offen offset:1024 lds\n\t"
    "v_mfma_f32_16x16x32_bf16 v[140:143], a[68:71], v[60:63], v[140:143]\n\t"
    "ds_read_b64_tr_b16 a[86:87], v12 offset:20992\n\t"
    "v_mfma_f32_16x16x32_bf16 v[140:143], a[64:67], v[44:47], v[140:143]\n\t"
    "s_waitcnt lgkmcnt(4)\n\t"
    "v_mfma_f32_16x16x32_bf16 v[156:159], a[72:75], v[44:47], v[156:159]\n\t"
    "ds_read_b64_tr_b16 a[88:89], v13 offset:16384\n\t"
    "v_mfma_f32_16x16x32_bf16 v[156:159], a[76:79], v[60:63], v[156:159]\n\t"
    "buffer_load_dwordx4 v230, s[48:51], s58 offen offset:2048 lds\n\t"
    "v_mfma_f32_16x16x32_bf16 v[152:155], a[76:79], v[56:59], v[152:155]\n\t"
    "ds_read_b64_tr_b16 a[90:91], v13 offset:16896\n\t"
    "v_mfma_f32_16x16x32_bf16 v[152:155], a[72:75], v[40:43], v[152:155]\n\t"
    "v_mfma_f32_16x16x32_bf16 v[148:151], a[72:75], v[36:39], v[148:151]\n\t"
    "ds_read_b64_tr_b16 a[92:93], v13 offset:20480\n\t"
    "v_mfma_f32_16x16x32_bf16 v[148:151], a[76:79], v[52:55], v[148:151]\n\t"
    "buffer_load_dwordx4 v231, s[48:51], s58 offen offset:3072 lds\n\t"
    "v_mfma_f32_16x16x32_bf16 v[144:147], a[76:79], v[48:51], v[144:147]\n\t"
    "ds_read_b64_tr_b16 a[94:95], v13 offset:20992\n\t"
    "v_mfma_f32_16x16x32_bf16 v[144:147], a[72:75], v[32:35], v[144:147]\n\t"
    "s_waitcnt lgkmcnt(4)\n\t"
    "v_mfma_f32_16x16x32_bf16 v[160:163], a[80:83], v[32:35], v[160:163]\n\t"
    "s_add_u32 s58, s73, 0\n\t"
    "s_add_u32 m0, s72, 32768\n\t"
    "ds_read_b64_tr_b16 a[64:65], v14 offset:16384\n\t"
    "v_mfma_f32_16x16x32_bf16 v[160:163], a[84:87], v[48:51], v[160:163]\n\t"
    "buffer_load_dwordx4 v224, s[48:51], s58 offen offset:0 lds\n\t"
    "v_mfma_f32_16x16x32_bf16 v[164:167], a[84:87], v[52:55], v[164:167]\n\t"
    "ds_read_b64_tr_b16 a[66:67], v14 offset:16896\n\t"
    "v_mfma_f32_16x16x32_bf16 v[164:167], a[80:83], v[36:39], v[164:167]\n\t"
    "v_mfma_f32_16x16x32_bf16 v[168:171], a[80:83], v[40:43], v[168:171]\n\t"
    "ds_read_b64_tr_b16 a[68:69], v14 offset:20480\n\t"
    "v_mfma_f32_16x16x32_bf16 v[168:171], a[84:87], v[56:59], v[168:171]\n\t"
    "v_mfma_f32_16x16x32_bf16 v[172:175], a[84:87], v[60:63], v[172:175]\n\t"
    "ds_read_b64_tr_b16 a[70:71], v14 offset:20992\n\t"
    "v_mfma_f32_16x16x32_bf16 v[172:175], a[80:83], v[44:47], v[172:175]\n\t"
    "s_waitcnt lgkmcnt(4)\n\t"
    "v_mfma_f32_16x16x32_bf16 v[188:191], a[88:91], v[44:47], v[188:191]\n\t"
    "ds_read_b64_tr_b16 a[72:73], v15 offset:16384\n\t"
    "v_mfma_f32_16x16x32_bf16 v[188:191], a[92:95], v[60:63], v[188:191]\n\t"
    "buffer_load_dwordx4 v225, s[48:51], s58 offen offset:1024 lds\n\t"
    "v_mfma_f32_16x16x32_bf16 v[184:187], a[92:95], v[56:59], v[184:187]\n\t"
    "s_add_u32 s58, s73, 196608\n\t"
    "ds_read_b64_tr_b16 a[74:75], v15 offset:16896\n\t"
    "v_mfma_f32_16x16x32_bf16 v[184:187], a[88:91], v[40:43], v[184:187]\n\t"
    "buffer_load_dwordx4 v226, s[48:51], s58 offen offset:2048 lds\n\t"
    "v_mfma_f32_16x16x32_bf16 v[180:183], a[88:91], v[36:39], v[180:183]\n\t"
    "ds_read_b64_tr_b16 a[76:77], v15 offset:20480\n\t"
    "v_mfma_f32_16x16x32_bf16 v[180:183], a[92:95], v[52:55], v[180:183]\n\t"
    "v_mfma_f32_16x16x32_bf16 v[176:179], a[92:95], v[48:51], v[176:179]\n\t"
    "ds_read_b64_tr_b16 a[78:79], v15 offset:20992\n\t"
    "v_mfma_f32_16x16x32_bf16 v[176:179], a[88:91], v[32:35], v[176:179]\n\t"
    "s_waitcnt lgkmcnt(4)\n\t"
    "v_mfma_f32_16x16x32_bf16 v[192:195], a[64:67], v[32:35], v[192:195]\n\t"
    "ds_read_b64_tr_b16 a[80:81], v14 offset:8192\n\t"
    "v_mfma_f32_16x16x32_bf16 v[192:195], a[68:71], v[48:51], v[192:195]\n\t"
    "buffer_load_dwordx4 v227, s[48:51], s58 offen offset:3072 lds\n\t"
    "v_mfma_f32_16x16x32_bf16 v[196:199], a[68:71], v[52:55], v[196:199]\n\t"
    "s_add_u32 s58, s73, 393216\n\t"
    "s_add_u32 m0, s72, 36864\n\t"
    "ds_read_b64_tr_b16 a[82:83], v14 offset:8704\n\t"
    "v_mfma_f32_16x16x32_bf16 v[196:199], a[64:67], v[36:39], v[196:199]\n\t"
    "buffer_load_dwordx4 v228, s[48:51], s58 offen offset:0 lds\n\t"
    "v_mfma_f32_16x16x32_bf16 v[200:203], a[64:67], v[40:43], v[200:203]\n\t"
    "ds_read_b64_tr_b16 a[84:85], v14 offset:12288\n\t"
    "v_mfma_f32_16x16x32_bf16 v[200:203], a[68:71], v[56:59], v[200:203]\n\t"
    "v_mfma_f32_16x16x32_bf16 v[204:207], a[68:71], v[60:63], v[204:207]\n\t"
    "ds_read_b64_tr_b16 a[86:87], v14 offset:12800\n\t"
    "v_mfma_f32_16x16x32_bf16 v[204:207], a[64:67], v[44:47], v[204:207]\n\t"
    "s_waitcnt lgkmcnt(4)\n\t"
    "v_mfma_f32_16x16x32_bf16 v[220:223], a[72:75], v[44:47], v[220:223]\n\t"
    "ds_read_b64_tr_b16 a[88:89], v15 offset:8192\n\t"
    "v_mfma_f32_16x16x32_bf16 v[220:223], a[76:79], v[60:63], v[220:223]\n\t"
    "buffer_load_dwordx4 v229, s[48:51], s58 offen offset:1024 lds\n\t"
    "v_mfma_f32_16x16x32_bf16 v[216:219], a[76:79], v[56:59], v[216:219]\n\t"
    "s_add_u32 s58, s73, 589824\n\t"
    "ds_read_b64_tr_b16 a[90:91], v15 offset:8704\n\t"
    "v_mfma_f32_16x16x32_bf16 v[216:219], a[72:75], v[40:43], v[216:219]\n\t"
    "buffer_load_dwordx4 v230, s[48:51], s58 offen offset:2048 lds\n\t"
    "v_mfma_f32_16x16x32_bf16 v[212:215], a[72:75], v[36:39], v[212:215]\n\t"
    "ds_read_b64_tr_b16 a[92:93], v15 offset:12288\n\t"
    "v_mfma_f32_16x16x32_bf16 v[212:215], a[76:79], v[52:55], v[212:215]\n\t"
    "v_mfma_f32_16x16x32_bf16 v[208:211], a[76:79], v[48:51], v[208:211]\n\t"
    "ds_read_b64_tr_b16 a[94:95], v15 offset:12800\n\t"
    "v_mfma_f32_16x16x32_bf16 v[208:211], a[72:75], v[32:35], v[208:211]\n\t"
    "s_waitcnt vmcnt(30) lgkmcnt(4)\n\t"
    "v_mfma_f32_16x16x32_bf16 v[128:131], a[80:83], v[64:67], v[128:131]\n\t"
    "ds_read_b64_tr_b16 a[64:65], v12 offset:24576\n\t"
    "v_mfma_f32_16x16x32_bf16 v[128:131], a[84:87], v[80:83], v[128:131]\n\t"
    "buffer_load_dwordx4 v231, s[48:51], s58 offen offset:3072 lds\n\t"
    "v_mfma_f32_16x16x32_bf16 v[132:135], a[84:87], v[84:87], v[132:135]\n\t"
    "s_lshl2_add_u32 s59, s82, 45056\n\t"
    "ds_read_b64_tr_b16 a[66:67], v12 offset:25088\n\t"
    "v_mfma_f32_16x16x32_bf16 v[132:135], a[80:83], v[68:71], v[132:135]\n\t"
    "buffer_load_dwordx4 a[160:163], v0, s[44:47], s59 offen offset:0 sc1\n\t"
    "v_mfma_f32_16x16x32_bf16 v[136:139], a[80:83], v[72:75], v[136:139]\n\t"
    "ds_read_b64_tr_b16 a[68:69], v12 offset:28672\n\t"
    "v_mfma_f32_16x16x32_bf16 v[136:139], a[84:87], v[88:91], v[136:139]\n\t"
    "v_mfma_f32_16x16x32_bf16 v[140:143], a[84:87], v[92:95], v[140:143]\n\t"
    "ds_read_b64_tr_b16 a[70:71], v12 offset:29184\n\t"
    "v_mfma_f32_16x16x32_bf16 v[140:143], a[80:83], v[76:79], v[140:143]\n\t"
    "s_waitcnt lgkmcnt(4)\n\t"
    "v_mfma_f32_16x16x32_bf16 v[156:159], a[88:91], v[76:79], v[156:159]\n\t"
    "ds_read_b64_tr_b16 a[72:73], v13 offset:24576\n\t"
    "v_mfma_f32_16x16x32_bf16 v[156:159], a[92:95], v[92:95], v[156:159]\n\t"
    "buffer_load_dwordx4 a[164:167], v1, s[44:47], s59 offen offset:0 sc1\n\t"
    "v_mfma_f32_16x16x32_bf16 v[152:155], a[92:95], v[88:91], v[152:155]\n\t"
    "ds_read_b64_tr_b16 a[74:75], v13 offset:25088\n\t"
    "v_mfma_f32_16x16x32_bf16 v[152:155], a[88:91], v[72:75], v[152:155]\n\t"
    "v_mfma_f32_16x16x32_bf16 v[148:151], a[88:91], v[68:71], v[148:151]\n\t"
    "ds_read_b64_tr_b16 a[76:77], v13 offset:28672\n\t"
    "v_mfma_f32_16x16x32_bf16 v[148:151], a[92:95], v[84:87], v[148:151]\n\t"
    "buffer_load_dwordx4 a[168:171], v2, s[44:47], s59 offen offset:0 sc1\n\t"
    "v_mfma_f32_16x16x32_bf16 v[144:147], a[92:95], v[80:83], v[144:147]\n\t"
    "ds_read_b64_tr_b16 a[78:79], v13 offset:29184\n\t"
    "v_mfma_f32_16x16x32_bf16 v[144:147], a[88:91], v[64:67], v[144:147]\n\t"
    "s_waitcnt lgkmcnt(4)\n\t"
    "v_mfma_f32_16x16x32_bf16 v[160:163], a[64:67], v[64:67], v[160:163]\n\t"
    "ds_read_b64_tr_b16 a[80:81], v14 offset:24576\n\t"
    "v_mfma_f32_16x16x32_bf16 v[160:163], a[68:71], v[80:83], v[160:163]\n\t"
    "buffer_load_dwordx4 a[172:175], v3, s[44:47], s59 offen offset:0 sc1\n\t"
    "v_mfma_f32_16x16x32_bf16 v[164:167], a[68:71], v[84:87], v[164:167]\n\t"
    "ds_read_b64_tr_b16 a[82:83], v14 offset:25088\n\t"
    "v_mfma_f32_16x16x32_bf16 v[164:167], a[64:67], v[68:71], v[164:167]\n\t"
    "v_mfma_f32_16x16x32_bf16 v[168:171], a[64:67], v[72:75], v[168:171]\n\t"
    "ds_read_b64_tr_b16 a[84:85], v14 offset:28672\n\t"
    "v_mfma_f32_16x16x32_bf16 v[168:171], a[68:71], v[88:91], v[168:171]\n\t"
    "buffer_load_dwordx4 a[176:179], v0, s[44:47], s59 offen offset:1024 sc1\n\t"
    "v_mfma_f32_16x16x32_bf16 v[172:175], a[68:71], v[92:95], v[172:175]\n\t"
    "ds_read_b64_tr_b16 a[86:87], v14 offset:29184\n\t"
    "v_mfma_f32_16x16x32_bf16 v[172:175], a[64:67], v[76:79], v[172:175]\n\t"
    "s_waitcnt lgkmcnt(4)\n\t"
    "v_mfma_f32_16x16x32_bf16 v[188:191], a[72:75], v[76:79], v[188:191]\n\t"
    "ds_read_b64_tr_b16 a[88:89], v15 offset:24576\n\t"
    "v_mfma_f32_16x16x32_bf16 v[188:191], a[76:79], v[92:95], v[188:191]\n\t"
    "buffer_load_dwordx4 a[180:183], v1, s[44:47], s59 offen offset:1024 sc1\n\t"
    "v_mfma_f32_16x16x32_bf16 v[184:187], a[76:79], v[88:91], v[184:187]\n\t"
    "ds_read_b64_tr_b16 a[90:91], v15 offset:25088\n\t"
    "v_mfma_f32_16x16x32_bf16 v[184:187], a[72:75], v[72:75], v[184:187]\n\t"
    "v_mfma_f32_16x16x32_bf16 v[180:183], a[72:75], v[68:71], v[180:183]\n\t"
    "ds_read_b64_tr_b16 a[92:93], v15 offset:28672\n\t"
    "v_mfma_f32_16x16x32_bf16 v[180:183], a[76:79], v[84:87], v[180:183]\n\t"
    "buffer_load_dwordx4 a[184:187], v2, s[44:47], s59 offen offset:1024 sc1\n\t"
    "v_mfma_f32_16x16x32_bf16 v[176:179], a[76:79], v[80:83], v[176:179]\n\t"
    "ds_read_b64_tr_b16 a[94:95], v15 offset:29184\n\t"
    "v_mfma_f32_16x16x32_bf16 v[176:179], a[72:75], v[64:67], v[176:179]\n\t"
    "s_waitcnt lgkmcnt(4)\n\t"
    "s_barrier\n\t"
    "v_mfma_f32_16x16x32_bf16 v[192:195], a[80:83], v[64:67], v[192:195]\n\t"
    "ds_read_b64_tr_b16 a[64:65], v10 offset:0\n\t"
    "v_mfma_f32_16x16x32_bf16 v[192:195], a[84:87], v[80:83], v[192:195]\n\t"
    "buffer_load_dwordx4 a[188:191], v3, s[44:47], s59 offen offset:1024 sc1\n\t"
    "v_mfma_f32_16x16x32_bf16 v[196:199], a[84:87], v[84:87], v[196:199]\n\t"
    "ds_read_b64_tr_b16 a[66:67], v10 offset:512\n\t"
    "v_mfma_f32_16x16x32_bf16 v[196:199], a[80:83], v[68:71], v[196:199]\n\t"
    "v_mfma_f32_16x16x32_bf16 v[200:203], a[80:83], v[72:75], v[200:203]\n\t"
    "ds_read_b64_tr_b16 a[68:69], v10 offset:4096\n\t"
    "v_mfma_f32_16x16x32_bf16 v[200:203], a[84:87], v[88:91], v[200:203]\n\t"
    "buffer_load_dwordx4 a[192:195], v0, s[44:47], s59 offen offset:2048 sc1\n\t"
    "v_mfma_f32_16x16x32_bf16 v[204:207], a[84:87], v[92:95], v[204:207]\n\t"
    "ds_read_b64_tr_b16 a[70:71], v10 offset:4608\n\t"
    "v_mfma_f32_16x16x32_bf16 v[204:207], a[80:83], v[76:79], v[204:207]\n\t"
    "s_waitcnt lgkmcnt(4)\n\t"
    "v_mfma_f32_16x16x32_bf16 v[220:223], a[88:91], v[76:79], v[220:223]\n\t"
    "ds_read_b64_tr_b16 a[72:73], v11 offset:0\n\t"
    "v_mfma_f32_16x16x32_bf16 v[220:223], a[92:95], v[92:95], v[220:223]\n\t"
    "v_mfma_f32_16x16x32_bf16 v[216:219], a[92:95], v[88:91], v[216:219]\n\t"
    "ds_read_b64_tr_b16 a[74:75], v11 offset:512\n\t"
    "v_mfma_f32_16x16x32_bf16 v[216:219], a[88:91], v[72:75], v[216:219]\n\t"
    "buffer_load_dwordx4 a[196:199], v1, s[44:47], s59 offen offset:2048 sc1\n\t"
    "v_mfma_f32_16x16x32_bf16 v[212:215], a[88:91], v[68:71], v[212:215]\n\t"
    "ds_read_b64_tr_b16 a[76:77], v11 offset:4096\n\t"
    "v_mfma_f32_16x16x32_bf16 v[212:215], a[92:95], v[84:87], v[212:215]\n\t"
    "v_mfma_f32_16x16x32_bf16 v[208:211], a[92:95], v[80:83], v[208:211]\n\t"
    "ds_read_b64_tr_b16 a[78:79], v11 offset:4608\n\t"
    "v_mfma_f32_16x16x32_bf16 v[208:211], a[88:91], v[64:67], v[208:211]\n\t"
    "s_waitcnt vmcnt(33) lgkmcnt(4)\n\t"
    "v_mfma_f32_16x16x32_bf16 v[128:131], a[64:67], a[96:99], v[128:131]\n\t"
    "ds_read_b64_tr_b16 a[80:81], v8 offset:16384\n\t"
    "v_mfma_f32_16x16x32_bf16 v[128:131], a[68:71], a[112:115], v[128:131]\n\t"
    "buffer_load_dwordx4 a[200:203], v2, s[44:47], s59 offen offset:2048 sc1\n\t"
    "v_mfma_f32_16x16x32_bf16 v[132:135], a[68:71], a[116:119], v[132:135]\n\t"
    "ds_read_b64_tr_b16 a[82:83], v8 offset:16896\n\t"
    "v_mfma_f32_16x16x32_bf16 v[132:135], a[64:67], a[100:103], v[132:135]\n\t"
    "buffer_load_dwordx4 a[204:207], v3, s[44:47], s59 offen offset:2048 sc1\n\t"
    "v_mfma_f32_16x16x32_bf16 v[136:139], a[64:67], a[104:107], v[136:139]\n\t"
    "ds_read_b64_tr_b16 a[84:85], v8 offset:20480\n\t"
    "v_mfma_f32_16x16x32_bf16 v[136:139], a[68:71], a[120:123], v[136:139]\n\t"
    "v_mfma_f32_16x16x32_bf16 v[140:143], a[68:71], a[124:127], v[140:143]\n\t"
    "ds_read_b64_tr_b16 a[86:87], v8 offset:20992\n\t"
    "v_mfma_f32_16x16x32_bf16 v[140:143], a[64:67], a[108:111], v[140:143]\n\t"
    "s_waitcnt lgkmcnt(4)\n\t"
    "v_mfma_f32_16x16x32_bf16 v[156:159], a[72:75], a[108:111], v[156:159]\n\t"
    "ds_read_b64_tr_b16 a[88:89], v9 offset:16384\n\t"
    "v_mfma_f32_16x16x32_bf16 v[156:159], a[76:79], a[124:127], v[156:159]\n\t"
    "buffer_load_dwordx4 a[208:211], v0, s[44:47], s59 offen offset:3072 sc1\n\t"
    "v_mfma_f32_16x16x32_bf16 v[152:155], a[76:79], a[120:123], v[152:155]\n\t"
    "ds_read_b64_tr_b16 a[90:91], v9 offset:16896\n\t"
    "v_mfma_f32_16x16x32_bf16 v[152:155], a[72:75], a[104:107], v[152:155]\n\t"
    "buffer_load_dwordx4 a[212:215], v1, s[44:47], s59 offen offset:3072 sc1\n\t"
    "v_mfma_f32_16x16x32_bf16 v[148:151], a[72:75], a[100:103], v[148:151]\n\t"
    "ds_read_b64_tr_b16 a[92:93], v9 offset:20480\n\t"
    "v_mfma_f32_16x16x32_bf16 v[148:151], a[76:79], a[116:119], v[148:151]\n\t"
    "v_mfma_f32_16x16x32_bf16 v[144:147], a[76:79], a[112:115], v[144:147]\n\t"
    "ds_read_b64_tr_b16 a[94:95], v9 offset:20992\n\t"
    "v_mfma_f32_16x16x32_bf16 v[144:147], a[72:75], a[96:99], v[144:147]\n\t"
    "s_waitcnt lgkmcnt(4)\n\t"
    "v_mfma_f32_16x16x32_bf16 v[160:163], a[80:83], a[96:99], v[160:163]\n\t"
    "ds_read_b64_tr_b16 a[64:65], v10 offset:16384\n\t"
    "v_mfma_f32_16x16x32_bf16 v[160:163], a[84:87], a[112:115], v[160:163]\n\t"
    "buffer_load_dwordx4 a[216:219], v2, s[44:47], s59 offen offset:3072 sc1\n\t"
    "v_mfma_f32_16x16x32_bf16 v[164:167], a[84:87], a[116:119], v[164:167]\n\t"
    "ds_read_b64_tr_b16 a[66:67], v10 offset:16896\n\t"
    "v_mfma_f32_16x16x32_bf16 v[164:167], a[80:83], a[100:103], v[164:167]\n\t"
    "buffer_load_dwordx4 a[220:223], v3, s[44:47], s59 offen offset:3072 sc1\n\t"
    "v_mfma_f32_16x16x32_bf16 v[168:171], a[80:83], a[104:107], v[168:171]\n\t"
    "ds_read_b64_tr_b16 a[68:69], v10 offset:20480\n\t"
    "v_mfma_f32_16x16x32_bf16 v[168:171], a[84:87], a[120:123], v[168:171]\n\t"
    "v_mfma_f32_16x16x32_bf16 v[172:175], a[84:87], a[124:127], v[172:175]\n\t"
    "ds_read_b64_tr_b16 a[70:71], v10 offset:20992\n\t"
    "v_mfma_f32_16x16x32_bf16 v[172:175], a[80:83], a[108:111], v[172:175]\n\t"
    "s_waitcnt lgkmcnt(4)\n\t"
    "v_mfma_f32_16x16x32_bf16 v[188:191], a[88:91], a[108:111], v[188:191]\n\t"
    "s_add_u32 s59, s82, 98304\n\t"
    "ds_read_b64_tr_b16 a[72:73], v11 offset:16384\n\t"
    "v_mfma_f32_16x16x32_bf16 v[188:191], a[92:95], a[124:127], v[188:191]\n\t"
    "buffer_load_dwordx4 a[0:3], v0, s[44:47], s59 offen offset:0 sc1\n\t"
    "v_mfma_f32_16x16x32_bf16 v[184:187], a[92:95], a[120:123], v[184:187]\n\t"
    "ds_read_b64_tr_b16 a[74:75], v11 offset:16896\n\t"
    "v_mfma_f32_16x16x32_bf16 v[184:187], a[88:91], a[104:107], v[184:187]\n\t"
    "v_mfma_f32_16x16x32_bf16 v[180:183], a[88:91], a[100:103], v[180:183]\n\t"
    "ds_read_b64_tr_b16 a[76:77], v11 offset:20480\n\t"
    "v_mfma_f32_16x16x32_bf16 v[180:183], a[92:95], a[116:119], v[180:183]\n\t"
    "buffer_load_dwordx4 a[4:7], v1, s[44:47], s59 offen offset:0 sc1\n\t"
    "v_mfma_f32_16x16x32_bf16 v[176:179], a[92:95], a[112:115], v[176:179]\n\t"
    "ds_read_b64_tr_b16 a[78:79], v11 offset:20992\n\t"
    "v_mfma_f32_16x16x32_bf16 v[176:179], a[88:91], a[96:99], v[176:179]\n\t"
    "s_waitcnt lgkmcnt(4)\n\t"
    "v_mfma_f32_16x16x32_bf16 v[192:195], a[64:67], a[96:99], v[192:195]\n\t"
    "ds_read_b64_tr_b16 a[80:81], v10 offset:8192\n\t"
    "v_mfma_f32_16x16x32_bf16 v[192:195], a[68:71], a[112:115], v[192:195]\n\t"
    "buffer_load_dwordx4 a[8:11], v2, s[44:47], s59 offen offset:0 sc1\n\t"
    "v_mfma_f32_16x16x32_bf16 v[196:199], a[68:71], a[116:119], v[196:199]\n\t"
    "ds_read_b64_tr_b16 a[82:83], v10 offset:8704\n\t"
    "v_mfma_f32_16x16x32_bf16 v[196:199], a[64:67], a[100:103], v[196:199]\n\t"
    "v_mfma_f32_16x16x32_bf16 v[200:203], a[64:67], a[104:107], v[200:203]\n\t"
    "ds_read_b64_tr_b16 a[84:85], v10 offset:12288\n\t"
    "v_mfma_f32_16x16x32_bf16 v[200:203], a[68:71], a[120:123], v[200:203]\n\t"
    "buffer_load_dwordx4 a[12:15], v3, s[44:47], s59 offen offset:0 sc1\n\t"
    "v_mfma_f32_16x16x32_bf16 v[204:207], a[68:71], a[124:127], v[204:207]\n\t"
    "ds_read_b64_tr_b16 a[86:87], v10 offset:12800\n\t"
    "v_mfma_f32_16x16x32_bf16 v[204:207], a[64:67], a[108:111], v[204:207]\n\t"
    "s_waitcnt lgkmcnt(4)\n\t"
    "v_mfma_f32_16x16x32_bf16 v[220:223], a[72:75], a[108:111], v[220:223]\n\t"
    "ds_read_b64_tr_b16 a[88:89], v11 offset:8192\n\t"
    "v_mfma_f32_16x16x32_bf16 v[220:223], a[76:79], a[124:127], v[220:223]\n\t"
    "buffer_load_dwordx4 a[16:19], v0, s[44:47], s59 offen offset:1024 sc1\n\t"
    "v_mfma_f32_16x16x32_bf16 v[216:219], a[76:79], a[120:123], v[216:219]\n\t"
    "ds_read_b64_tr_b16 a[90:91], v11 offset:8704\n\t"
    "v_mfma_f32_16x16x32_bf16 v[216:219], a[72:75], a[104:107], v[216:219]\n\t"
    "v_mfma_f32_16x16x32_bf16 v[212:215], a[72:75], a[100:103], v[212:215]\n\t"
    "ds_read_b64_tr_b16 a[92:93], v11 offset:12288\n\t"
    "v_mfma_f32_16x16x32_bf16 v[212:215], a[76:79], a[116:119], v[212:215]\n\t"
    "buffer_load_dwordx4 a[20:23], v1, s[44:47], s59 offen offset:1024 sc1\n\t"
    "v_mfma_f32_16x16x32_bf16 v[208:211], a[76:79], a[112:115], v[208:211]\n\t"
    "ds_read_b64_tr_b16 a[94:95], v11 offset:12800\n\t"
    "v_mfma_f32_16x16x32_bf16 v[208:211], a[72:75], a[96:99], v[208:211]\n\t"
    "s_waitcnt vmcnt(37) lgkmcnt(4)\n\t"
    "v_mfma_f32_16x16x32_bf16 v[128:131], a[80:83], a[128:131], v[128:131]\n\t"
    "ds_read_b64_tr_b16 a[64:65], v8 offset:24576\n\t"
    "v_mfma_f32_16x16x32_bf16 v[128:131], a[84:87], a[144:147], v[128:131]\n\t"
    "buffer_load_dwordx4 a[24:27], v2, s[44:47], s59 offen offset:1024 sc1\n\t"
    "v_mfma_f32_16x16x32_bf16 v[132:135], a[84:87], a[148:151], v[132:135]\n\t"
    "ds_read_b64_tr_b16 a[66:67], v8 offset:25088\n\t"
    "v_mfma_f32_16x16x32_bf16 v[132:135], a[80:83], a[132:135], v[132:135]\n\t"
    "v_mfma_f32_16x16x32_bf16 v[136:139], a[80:83], a[136:139], v[136:139]\n\t"
    "ds_read_b64_tr_b16 a[68:69], v8 offset:28672\n\t"
    "v_mfma_f32_16x16x32_bf16 v[136:139], a[84:87], a[152:155], v[136:139]\n\t"
    "buffer_load_dwordx4 a[28:31], v3, s[44:47], s59 offen offset:1024 sc1\n\t"
    "v_mfma_f32_16x16x32_bf16 v[140:143], a[84:87], a[156:159], v[140:143]\n\t"
    "ds_read_b64_tr_b16 a[70:71], v8 offset:29184\n\t"
    "v_mfma_f32_16x16x32_bf16 v[140:143], a[80:83], a[140:143], v[140:143]\n\t"
    "s_waitcnt lgkmcnt(4)\n\t"
    "v_mfma_f32_16x16x32_bf16 v[156:159], a[88:91], a[140:143], v[156:159]\n\t"
    "ds_read_b64_tr_b16 a[72:73], v9 offset:24576\n\t"
    "v_mfma_f32_16x16x32_bf16 v[156:159], a[92:95], a[156:159], v[156:159]\n\t"
    "buffer_load_dwordx4 a[32:35], v0, s[44:47], s59 offen offset:2048 sc1\n\t"
    "v_mfma_f32_16x16x32_bf16 v[152:155], a[92:95], a[152:155], v[152:155]\n\t"
    "ds_read_b64_tr_b16 a[74:75], v9 offset:25088\n\t"
    "v_mfma_f32_16x16x32_bf16 v[152:155], a[88:91], a[136:139], v[152:155]\n\t"
    "v_mfma_f32_16x16x32_bf16 v[148:151], a[88:91], a[132:135], v[148:151]\n\t"
    "ds_read_b64_tr_b16 a[76:77], v9 offset:28672\n\t"
    "v_mfma_f32_16x16x32_bf16 v[148:151], a[92:95], a[148:151], v[148:151]\n\t"
    "v_mfma_f32_16x16x32_bf16 v[144:147], a[92:95], a[144:147], v[144:147]\n\t"
    "ds_read_b64_tr_b16 a[78:79], v9 offset:29184\n\t"
    "v_mfma_f32_16x16x32_bf16 v[144:147], a[88:91], a[128:131], v[144:147]\n\t"
    "s_waitcnt lgkmcnt(4)\n\t"
    "v_mfma_f32_16x16x32_bf16 v[160:163], a[64:67], a[128:131], v[160:163]\n\t"
    "ds_read_b64_tr_b16 a[80:81], v10 offset:24576\n\t"
    "v_mfma_f32_16x16x32_bf16 v[160:163], a[68:71], a[144:147], v[160:163]\n\t"
    "buffer_load_dwordx4 a[36:39], v1, s[44:47], s59 offen offset:2048 sc1\n\t"
    "v_mfma_f32_16x16x32_bf16 v[164:167], a[68:71], a[148:151], v[164:167]\n\t"
    "ds_read_b64_tr_b16 a[82:83], v10 offset:25088\n\t"
    "v_mfma_f32_16x16x32_bf16 v[164:167], a[64:67], a[132:135], v[164:167]\n\t"
    "buffer_load_dwordx4 a[40:43], v2, s[44:47], s59 offen offset:2048 sc1\n\t"
    "v_mfma_f32_16x16x32_bf16 v[168:171], a[64:67], a[136:139], v[168:171]\n\t"
    "ds_read_b64_tr_b16 a[84:85], v10 offset:28672\n\t"
    "v_mfma_f32_16x16x32_bf16 v[168:171], a[68:71], a[152:155], v[168:171]\n\t"
    "v_mfma_f32_16x16x32_bf16 v[172:175], a[68:71], a[156:159], v[172:175]\n\t"
    "ds_read_b64_tr_b16 a[86:87], v10 offset:29184\n\t"
    "v_mfma_f32_16x16x32_bf16 v[172:175], a[64:67], a[140:143], v[172:175]\n\t"
    "s_waitcnt lgkmcnt(4)\n\t"
    "v_mfma_f32_16x16x32_bf16 v[188:191], a[72:75], a[140:143], v[188:191]\n\t"
    "ds_read_b64_tr_b16 a[88:89], v11 offset:24576\n\t"
    "v_mfma_f32_16x16x32_bf16 v[188:191], a[76:79], a[156:159], v[188:191]\n\t"
    "buffer_load_dwordx4 a[44:47], v3, s[44:47], s59 offen offset:2048 sc1\n\t"
    "v_mfma_f32_16x16x32_bf16 v[184:187], a[76:79], a[152:155], v[184:187]\n\t"
    "ds_read_b64_tr_b16 a[90:91], v11 offset:25088\n\t"
    "v_mfma_f32_16x16x32_bf16 v[184:187], a[72:75], a[136:139], v[184:187]\n\t"
    "buffer_load_dwordx4 a[48:51], v0, s[44:47], s59 offen offset:3072 sc1\n\t"
    "v_mfma_f32_16x16x32_bf16 v[180:183], a[72:75], a[132:135], v[180:183]\n\t"
    "ds_read_b64_tr_b16 a[92:93], v11 offset:28672\n\t"
    "v_mfma_f32_16x16x32_bf16 v[180:183], a[76:79], a[148:151], v[180:183]\n\t"
    "v_mfma_f32_16x16x32_bf16 v[176:179], a[76:79], a[144:147], v[176:179]\n\t"
    "ds_read_b64_tr_b16 a[94:95], v11 offset:29184\n\t"
    "v_mfma_f32_16x16x32_bf16 v[176:179], a[72:75], a[128:131], v[176:179]\n\t"
    "s_waitcnt lgkmcnt(4)\n\t"
    "s_waitcnt vmcnt(29)\n\t"
    "s_barrier\n\t"
    "v_mfma_f32_16x16x32_bf16 v[192:195], a[80:83], a[128:131], v[192:195]\n\t"
    "ds_read_b64_tr_b16 a[64:65], v6 offset:0\n\t"
    "v_mfma_f32_16x16x32_bf16 v[192:195], a[84:87], a[144:147], v[192:195]\n\t"
    "buffer_load_dwordx4 a[52:55], v1, s[44:47], s59 offen offset:3072 sc1\n\t"
    "v_mfma_f32_16x16x32_bf16 v[196:199], a[84:87], a[148:151], v[196:199]\n\t"
    "ds_read_b64_tr_b16 a[66:67], v6 offset:512\n\t"
    "v_mfma_f32_16x16x32_bf16 v[196:199], a[80:83], a[132:135], v[196:199]\n\t"
    "buffer_load_dwordx4 a[56:59], v2, s[44:47], s59 offen offset:3072 sc1\n\t"
    "v_mfma_f32_16x16x32_bf16 v[200:203], a[80:83], a[136:139], v[200:203]\n\t"
    "ds_read_b64_tr_b16 a[68:69], v6 offset:4096\n\t"
    "v_mfma_f32_16x16x32_bf16 v[200:203], a[84:87], a[152:155], v[200:203]\n\t"
    "v_mfma_f32_16x16x32_bf16 v[204:207], a[84:87], a[156:159], v[204:207]\n\t"
    "ds_read_b64_tr_b16 a[70:71], v6 offset:4608\n\t"
    "v_mfma_f32_16x16x32_bf16 v[204:207], a[80:83], a[140:143], v[204:207]\n\t"
    "s_waitcnt lgkmcnt(4)\n\t"
    "v_mfma_f32_16x16x32_bf16 v[220:223], a[88:91], a[140:143], v[220:223]\n\t"
    "ds_read_b64_tr_b16 a[72:73], v6 offset:8192\n\t"
    "v_mfma_f32_16x16x32_bf16 v[220:223], a[92:95], a[156:159], v[220:223]\n\t"
    "buffer_load_dwordx4 a[60:63], v3, s[44:47], s59 offen offset:3072 sc1\n\t"
    "v_mfma_f32_16x16x32_bf16 v[216:219], a[92:95], a[152:155], v[216:219]\n\t"
    "ds_read_b64_tr_b16 a[74:75], v6 offset:8704\n\t"
    "v_mfma_f32_16x16x32_bf16 v[216:219], a[88:91], a[136:139], v[216:219]\n\t"
    "v_mfma_f32_16x16x32_bf16 v[212:215], a[88:91], a[132:135], v[212:215]\n\t"
    "ds_read_b64_tr_b16 a[76:77], v6 offset:12288\n\t"
    "v_mfma_f32_16x16x32_bf16 v[212:215], a[92:95], a[148:151], v[212:215]\n\t"
    "v_mfma_f32_16x16x32_bf16 v[208:211], a[92:95], a[144:147], v[208:211]\n\t"
    "ds_read_b64_tr_b16 a[78:79], v6 offset:12800\n\t"
    "v_mfma_f32_16x16x32_bf16 v[208:211], a[88:91], a[128:131], v[208:211]\n\t"
    "s_waitcnt vmcnt(24) lgkmcnt(4)\n\t"
    "v_mfma_f32_16x16x32_bf16 v[128:131], a[64:67], a[160:163], v[128:131]\n\t"
    "ds_read_b64_tr_b16 a[80:81], v6 offset:32768\n\t"
    "v_mfma_f32_16x16x32_bf16 v[128:131], a[68:71], a[176:179], v[128:131]\n\t"
    "v_mfma_f32_16x16x32_bf16 v[132:135], a[68:71], a[180:183], v[132:135]\n\t"
    "ds_read_b64_tr_b16 a[82:83], v6 offset:33280\n\t"
    "v_mfma_f32_16x16x32_bf16 v[132:135], a[64:67], a[164:167], v[132:135]\n\t"
    "v_mfma_f32_16x16x32_bf16 v[136:139], a[64:67], a[168:171], v[136:139]\n\t"
    "ds_read_b64_tr_b16 a[84:85], v6 offset:36864\n\t"
    "v_mfma_f32_16x16x32_bf16 v[136:139], a[68:71], a[184:187], v[136:139]\n\t"
    "v_mfma_f32_16x16x32_bf16 v[140:143], a[68:71], a[188:191], v[140:143]\n\t"
    "ds_read_b64_tr_b16 a[86:87], v6 offset:37376\n\t"
    "v_mfma_f32_16x16x32_bf16 v[140:143], a[64:67], a[172:175], v[140:143]\n\t"
    "s_waitcnt vmcnt(16) lgkmcnt(4)\n\t"
    "v_mfma_f32_16x16x32_bf16 v[140:143], a[72:75], a[204:207], v[140:143]\n\t"
    "ds_read_b64_tr_b16 a[88:89], v6 offset:40960\n\t"
    "v_mfma_f32_16x16x32_bf16 v[140:143], a[76:79], a[220:223], v[140:143]\n\t"
    "v_mfma_f32_16x16x32_bf16 v[136:139], a[76:79], a[216:219], v[136:139]\n\t"
    "ds_read_b64_tr_b16 a[90:91], v6 offset:41472\n\t"
    "v_mfma_f32_16x16x32_bf16 v[136:139], a[72:75], a[200:203], v[136:139]\n\t"
    "v_mfma_f32_16x16x32_bf16 v[132:135], a[72:75], a[196:199], v[132:135]\n\t"
    "ds_read_b64_tr_b16 a[92:93], v6 offset:45056\n\t"
    "v_mfma_f32_16x16x32_bf16 v[132:135], a[76:79], a[212:215], v[132:135]\n\t"
    "v_mfma_f32_16x16x32_bf16 v[128:131], a[76:79], a[208:211], v[128:131]\n\t"
    "ds_read_b64_tr_b16 a[94:95], v6 offset:45568\n\t"
    "v_mfma_f32_16x16x32_bf16 v[128:131], a[72:75], a[192:195], v[128:131]\n\t"
    "s_waitcnt vmcnt(8) lgkmcnt(4)\n\t"
    "v_mfma_f32_16x16x32_bf16 v[128:131], a[80:83], a[0:3], v[128:131]\n\t"
    "ds_read_b64_tr_b16 a[64:65], v7 offset:0\n\t"
    "v_mfma_f32_16x16x32_bf16 v[128:131], a[84:87], a[16:19], v[128:131]\n\t"
    "v_mfma_f32_16x16x32_bf16 v[132:135], a[84:87], a[20:23], v[132:135]\n\t"
    "ds_read_b64_tr_b16 a[66:67], v7 offset:512\n\t"
    "v_mfma_f32_16x16x32_bf16 v[132:135], a[80:83], a[4:7], v[132:135]\n\t"
    "v_mfma_f32_16x16x32_bf16 v[136:139], a[80:83], a[8:11], v[136:139]\n\t"
    "ds_read_b64_tr_b16 a[68:69], v7 offset:4096\n\t"
    "v_mfma_f32_16x16x32_bf16 v[136:139], a[84:87], a[24:27], v[136:139]\n\t"
    "v_mfma_f32_16x16x32_bf16 v[140:143], a[84:87], a[28:31], v[140:143]\n\t"
    "ds_read_b64_tr_b16 a[70:71], v7 offset:4608\n\t"
    "v_mfma_f32_16x16x32_bf16 v[140:143], a[80:83], a[12:15], v[140:143]\n\t"
    "s_waitcnt vmcnt(0) lgkmcnt(4)\n\t"
    "v_mfma_f32_16x16x32_bf16 v[140:143], a[88:91], a[44:47], v[140:143]\n\t"
    "ds_read_b64_tr_b16 a[72:73], v7 offset:8192\n\t"
    "v_mfma_f32_16x16x32_bf16 v[140:143], a[92:95], a[60:63], v[140:143]\n\t"
    "s_or_b32 s61, s2, s3\n\t"
    "s_cbranch_scc1 EPA_%=\n\t"
    "v_lshlrev_b32 v248, 5, v255\n\t"
    "s_mov_b32 s86, 33554688\n\t"
    "buffer_load_dwordx4 v[240:243], v248, s[44:47], s86 offen sc1\n\t"
    "buffer_load_dwordx4 v[244:247], v248, s[44:47], s86 offen offset:16 sc1\n\t"
    "EPA_%=:\n\t"
    "v_mfma_f32_16x16x32_bf16 v[136:139], a[92:95], a[56:59], v[136:139]\n\t"
    "ds_read_b64_tr_b16 a[74:75], v7 offset:8704\n\t"
    "v_mfma_f32_16x16x32_bf16 v[136:139], a[88:91], a[40:43], v[136:139]\n\t"
    "v_mfma_f32_16x16x32_bf16 v[132:135], a[88:91], a[36:39], v[132:135]\n\t"
    "ds_read_b64_tr_b16 a[76:77], v7 offset:12288\n\t"
    "v_mfma_f32_16x16x32_bf16 v[132:135], a[92:95], a[52:55], v[132:135]\n\t"
    "v_mfma_f32_16x16x32_bf16 v[128:131], a[92:95], a[48:51], v[128:131]\n\t"
    "ds_read_b64_tr_b16 a[78:79], v7 offset:12800\n\t"
    "v_mfma_f32_16x16x32_bf16 v[128:131], a[88:91], a[32:35], v[128:131]\n\t"
    "s_waitcnt lgkmcnt(4)\n\t"
    "v_mfma_f32_16x16x32_bf16 v[144:147], a[64:67], a[160:163], v[144:147]\n\t"
    "ds_read_b64_tr_b16 a[80:81], v7 offset:32768\n\t"
    "v_mfma_f32_16x16x32_bf16 v[144:147], a[68:71], a[176:179], v[144:147]\n\t"
    "v_mfma_f32_16x16x32_bf16 v[148:151], a[68:71], a[180:183], v[148:151]\n\t"
    "ds_read_b64_tr_b16 a[82:83], v7 offset:33280\n\t"
    "v_mfma_f32_16x16x32_bf16 v[148:151], a[64:67], a[164:167], v[148:151]\n\t"
    "v_mfma_f32_16x16x32_bf16 v[152:155], a[64:67], a[168:171], v[152:155]\n\t"
    "ds_read_b64_tr_b16 a[84:85], v7 offset:36864\n\t"
    "v_mfma_f32_16x16x32_bf16 v[152:155], a[68:71], a[184:187], v[152:155]\n\t"
    "v_cvt_pk_bf16_f32 v128, v128, v129\n\t"
    "v_cvt_pk_bf16_f32 v129, v130, v131\n\t"
    "v_mfma_f32_16x16x32_bf16 v[156:159], a[68:71], a[188:191], v[156:159]\n\t"
    "v_cvt_pk_bf16_f32 v130, v132, v133\n\t"
    "v_cvt_pk_bf16_f32 v131, v134, v135\n\t"
    "ds_read_b64_tr_b16 a[86:87], v7 offset:37376\n\t"
    "v_mfma_f32_16x16x32_bf16 v[156:159], a[64:67], a[172:175], v[156:159]\n\t"
    "v_cvt_pk_bf16_f32 v132, v136, v137\n\t"
    "v_cvt_pk_bf16_f32 v133, v138, v139\n\t"
    "s_waitcnt lgkmcnt(4)\n\t"
    "v_mfma_f32_16x16x32_bf16 v[156:159], a[72:75], a[204:207], v[156:159]\n\t"
    "v_cvt_pk_bf16_f32 v134, v140, v141\n\t"
    "v_cvt_pk_bf16_f32 v135, v142, v143\n\t"
    "ds_read_b64_tr_b16 a[88:89], v7 offset:40960\n\t"
    "v_mfma_f32_16x16x32_bf16 v[156:159], a[76:79], a[220:223], v[156:159]\n\t"
    "buffer_store_dwordx4 v[128:131], v16, s[44:47], s80 offen offset:0 sc1\n\t"
    "v_mfma_f32_16x16x32_bf16 v[152:155], a[76:79], a[216:219], v[152:155]\n\t"
    "ds_read_b64_tr_b16 a[90:91], v7 offset:41472\n\t"
    "v_mfma_f32_16x16x32_bf16 v[152:155], a[72:75], a[200:203], v[152:155]\n\t"
    "buffer_store_dwordx4 v[132:135], v16, s[44:47], s80 offen offset:1024 sc1\n\t"
    "v_mfma_f32_16x16x32_bf16 v[148:151], a[72:75], a[196:199], v[148:151]\n\t"
    "ds_read_b64_tr_b16 a[92:93], v7 offset:45056\n\t"
    "v_mfma_f32_16x16x32_bf16 v[148:151], a[76:79], a[212:215], v[148:151]\n\t"
    "v_mfma_f32_16x16x32_bf16 v[144:147], a[76:79], a[208:211], v[144:147]\n\t"
    "ds_read_b64_tr_b16 a[94:95], v7 offset:45568\n\t"
    "v_mfma_f32_16x16x32_bf16 v[144:147], a[72:75], a[192:195], v[144:147]\n\t"
    "s_waitcnt lgkmcnt(4)\n\t"
    "v_mfma_f32_16x16x32_bf16 v[144:147], a[80:83], a[0:3], v[144:147]\n\t"
    "ds_read_b64_tr_b16 a[64:65], v4 offset:16384\n\t"
    "v_mfma_f32_16x16x32_bf16 v[144:147], a[84:87], a[16:19], v[144:147]\n\t"
    "v_mfma_f32_16x16x32_bf16 v[148:151], a[84:87], a[20:23], v[148:151]\n\t"
    "ds_read_b64_tr_b16 a[66:67], v4 offset:16896\n\t"
    "v_mfma_f32_16x16x32_bf16 v[148:151], a[80:83], a[4:7], v[148:151]\n\t"
    "v_mfma_f32_16x16x32_bf16 v[152:155], a[80:83], a[8:11], v[152:155]\n\t"
    "ds_read_b64_tr_b16 a[68:69], v4 offset:20480\n\t"
    "v_mfma_f32_16x16x32_bf16 v[152:155], a[84:87], a[24:27], v[152:155]\n\t"
    "v_mfma_f32_16x16x32_bf16 v[156:159], a[84:87], a[28:31], v[156:159]\n\t"
    "ds_read_b64_tr_b16 a[70:71], v4 offset:20992\n\t"
    "v_mfma_f32_16x16x32_bf16 v[156:159], a[80:83], a[12:15], v[156:159]\n\t"
    "s_waitcnt lgkmcnt(4)\n\t"
    "v_mfma_f32_16x16x32_bf16 v[156:159], a[88:91], a[44:47], v[156:159]\n\t"
    "ds_read_b64_tr_b16 a[72:73], v4 offset:24576\n\t"
    "v_mfma_f32_16x16x32_bf16 v[156:159], a[92:95], a[60:63], v[156:159]\n\t"
    "v_mfma_f32_16x16x32_bf16 v[152:155], a[92:95], a[56:59], v[152:155]\n\t"
    "ds_read_b64_tr_b16 a[74:75], v4 offset:25088\n\t"
    "v_mfma_f32_16x16x32_bf16 v[152:155], a[88:91], a[40:43], v[152:155]\n\t"
    "v_mfma_f32_16x16x32_bf16 v[148:151], a[88:91], a[36:39], v[148:151]\n\t"
    "ds_read_b64_tr_b16 a[76:77], v4 offset:28672\n\t"
    "v_mfma_f32_16x16x32_bf16 v[148:151], a[92:95], a[52:55], v[148:151]\n\t"
    "v_mfma_f32_16x16x32_bf16 v[144:147], a[92:95], a[48:51], v[144:147]\n\t"
    "ds_read_b64_tr_b16 a[78:79], v4 offset:29184\n\t"
    "v_mfma_f32_16x16x32_bf16 v[144:147], a[88:91], a[32:35], v[144:147]\n\t"
    "s_waitcnt lgkmcnt(4)\n\t"
    "v_mfma_f32_16x16x32_bf16 v[160:163], a[64:67], a[160:163], v[160:163]\n\t"
    "ds_read_b64_tr_b16 a[80:81], v4 offset:49152\n\t"
    "v_mfma_f32_16x16x32_bf16 v[160:163], a[68:71], a[176:179], v[160:163]\n\t"
    "v_mfma_f32_16x16x32_bf16 v[164:167], a[68:71], a[180:183], v[164:167]\n\t"
    "ds_read_b64_tr_b16 a[82:83], v4 offset:49664\n\t"
    "v_mfma_f32_16x16x32_bf16 v[164:167], a[64:67], a[164:167], v[164:167]\n\t"
    "v_mfma_f32_16x16x32_bf16 v[168:171], a[64:67], a[168:171], v[168:171]\n\t"
    "ds_read_b64_tr_b16 a[84:85], v4 offset:53248\n\t"
    "v_mfma_f32_16x16x32_bf16 v[168:171], a[68:71], a[184:187], v[168:171]\n\t"
    "v_cvt_pk_bf16_f32 v144, v144, v145\n\t"
    "v_cvt_pk_bf16_f32 v145, v146, v147\n\t"
    "v_mfma_f32_16x16x32_bf16 v[172:175], a[68:71], a[188:191], v[172:175]\n\t"
    "v_cvt_pk_bf16_f32 v146, v148, v149\n\t"
    "v_cvt_pk_bf16_f32 v147, v150, v151\n\t"
    "ds_read_b64_tr_b16 a[86:87], v4 offset:53760\n\t"
    "v_mfma_f32_16x16x32_bf16 v[172:175], a[64:67], a[172:175], v[172:175]\n\t"
    "v_cvt_pk_bf16_f32 v148, v152, v153\n\t"
    "v_cvt_pk_bf16_f32 v149, v154, v155\n\t"
    "s_waitcnt lgkmcnt(4)\n\t"
    "v_mfma_f32_16x16x32_bf16 v[172:175], a[72:75], a[204:207], v[172:175]\n\t"
    "v_cvt_pk_bf16_f32 v150, v156, v157\n\t"
    "v_cvt_pk_bf16_f32 v151, v158, v159\n\t"
    "ds_read_b64_tr_b16 a[88:89], v4 offset:57344\n\t"
    "v_mfma_f32_16x16x32_bf16 v[172:175], a[76:79], a[220:223], v[172:175]\n\t"
    "buffer_store_dwordx4 v[144:147], v16, s[44:47], s80 offen offset:2048 sc1\n\t"
    "v_mfma_f32_16x16x32_bf16 v[168:171], a[76:79], a[216:219], v[168:171]\n\t"
    "ds_read_b64_tr_b16 a[90:91], v4 offset:57856\n\t"
    "v_mfma_f32_16x16x32_bf16 v[168:171], a[72:75], a[200:203], v[168:171]\n\t"
    "buffer_store_dwordx4 v[148:151], v16, s[44:47], s80 offen offset:3072 sc1\n\t"
    "v_mfma_f32_16x16x32_bf16 v[164:167], a[72:75], a[196:199], v[164:167]\n\t"
    "ds_read_b64_tr_b16 a[92:93], v4 offset:61440\n\t"
    "v_mfma_f32_16x16x32_bf16 v[164:167], a[76:79], a[212:215], v[164:167]\n\t"
    "v_mfma_f32_16x16x32_bf16 v[160:163], a[76:79], a[208:211], v[160:163]\n\t"
    "ds_read_b64_tr_b16 a[94:95], v4 offset:61952\n\t"
    "v_mfma_f32_16x16x32_bf16 v[160:163], a[72:75], a[192:195], v[160:163]\n\t"
    "s_waitcnt lgkmcnt(4)\n\t"
    "v_mfma_f32_16x16x32_bf16 v[160:163], a[80:83], a[0:3], v[160:163]\n\t"
    "ds_read_b64_tr_b16 a[64:65], v5 offset:16384\n\t"
    "v_mfma_f32_16x16x32_bf16 v[160:163], a[84:87], a[16:19], v[160:163]\n\t"
    "v_mfma_f32_16x16x32_bf16 v[164:167], a[84:87], a[20:23], v[164:167]\n\t"
    "ds_read_b64_tr_b16 a[66:67], v5 offset:16896\n\t"
    "v_mfma_f32_16x16x32_bf16 v[164:167], a[80:83], a[4:7], v[164:167]\n\t"
    "v_mfma_f32_16x16x32_bf16 v[168:171], a[80:83], a[8:11], v[168:171]\n\t"
    "ds_read_b64_tr_b16 a[68:69], v5 offset:20480\n\t"
    "v_mfma_f32_16x16x32_bf16 v[168:171], a[84:87], a[24:27], v[168:171]\n\t"
    "v_mfma_f32_16x16x32_bf16 v[172:175], a[84:87], a[28:31], v[172:175]\n\t"
    "ds_read_b64_tr_b16 a[70:71], v5 offset:20992\n\t"
    "v_mfma_f32_16x16x32_bf16 v[172:175], a[80:83], a[12:15], v[172:175]\n\t"
    "s_waitcnt lgkmcnt(4)\n\t"
    "v_mfma_f32_16x16x32_bf16 v[172:175], a[88:91], a[44:47], v[172:175]\n\t"
    "ds_read_b64_tr_b16 a[72:73], v5 offset:24576\n\t"
    "v_mfma_f32_16x16x32_bf16 v[172:175], a[92:95], a[60:63], v[172:175]\n\t"
    "v_mfma_f32_16x16x32_bf16 v[168:171], a[92:95], a[56:59], v[168:171]\n\t"
    "ds_read_b64_tr_b16 a[74:75], v5 offset:25088\n\t"
    "v_mfma_f32_16x16x32_bf16 v[168:171], a[88:91], a[40:43], v[168:171]\n\t"
    "v_mfma_f32_16x16x32_bf16 v[164:167], a[88:91], a[36:39], v[164:167]\n\t"
    "ds_read_b64_tr_b16 a[76:77], v5 offset:28672\n\t"
    "v_mfma_f32_16x16x32_bf16 v[164:167], a[92:95], a[52:55], v[164:167]\n\t"
    "v_mfma_f32_16x16x32_bf16 v[160:163], a[92:95], a[48:51], v[160:163]\n\t"
    "ds_read_b64_tr_b16 a[78:79], v5 offset:29184\n\t"
    "v_mfma_f32_16x16x32_bf16 v[160:163], a[88:91], a[32:35], v[160:163]\n\t"
    "s_add_u32 s80, s80, 16512\n\t"
    "s_waitcnt lgkmcnt(4)\n\t"
    "v_mfma_f32_16x16x32_bf16 v[176:179], a[64:67], a[160:163], v[176:179]\n\t"
    "ds_read_b64_tr_b16 a[80:81], v5 offset:49152\n\t"
    "v_mfma_f32_16x16x32_bf16 v[176:179], a[68:71], a[176:179], v[176:179]\n\t"
    "v_mfma_f32_16x16x32_bf16 v[180:183], a[68:71], a[180:183], v[180:183]\n\t"
    "ds_read_b64_tr_b16 a[82:83], v5 offset:49664\n\t"
    "v_mfma_f32_16x16x32_bf16 v[180:183], a[64:67], a[164:167], v[180:183]\n\t"
    "v_mfma_f32_16x16x32_bf16 v[184:187], a[64:67], a[168:171], v[184:187]\n\t"
    "ds_read_b64_tr_b16 a[84:85], v5 offset:53248\n\t"
    "v_mfma_f32_16x16x32_bf16 v[184:187], a[68:71], a[184:187], v[184:187]\n\t"
    "v_cvt_pk_bf16_f32 v160, v160, v161\n\t"
    "v_cvt_pk_bf16_f32 v161, v162, v163\n\t"
    "v_mfma_f32_16x16x32_bf16 v[188:191], a[68:71], a[188:191], v[188:191]\n\t"
    "v_cvt_pk_bf16_f32 v162, v164, v165\n\t"
    "v_cvt_pk_bf16_f32 v163, v166, v167\n\t"
    "ds_read_b64_tr_b16 a[86:87], v5 offset:53760\n\t"
    "v_mfma_f32_16x16x32_bf16 v[188:191], a[64:67], a[172:175], v[188:191]\n\t"
    "v_cvt_pk_bf16_f32 v164, v168, v169\n\t"
    "v_cvt_pk_bf16_f32 v165, v170, v171\n\t"
    "s_waitcnt lgkmcnt(4)\n\t"
    "v_mfma_f32_16x16x32_bf16 v[188:191], a[72:75], a[204:207], v[188:191]\n\t"
    "v_cvt_pk_bf16_f32 v166, v172, v173\n\t"
    "v_cvt_pk_bf16_f32 v167, v174, v175\n\t"
    "ds_read_b64_tr_b16 a[88:89], v5 offset:57344\n\t"
    "v_mfma_f32_16x16x32_bf16 v[188:191], a[76:79], a[220:223], v[188:191]\n\t"
    "buffer_store_dwordx4 v[160:163], v16, s[44:47], s80 offen offset:0 sc1\n\t"
    "v_mfma_f32_16x16x32_bf16 v[184:187], a[76:79], a[216:219], v[184:187]\n\t"
    "ds_read_b64_tr_b16 a[90:91], v5 offset:57856\n\t"
    "v_mfma_f32_16x16x32_bf16 v[184:187], a[72:75], a[200:203], v[184:187]\n\t"
    "buffer_store_dwordx4 v[164:167], v16, s[44:47], s80 offen offset:1024 sc1\n\t"
    "v_mfma_f32_16x16x32_bf16 v[180:183], a[72:75], a[196:199], v[180:183]\n\t"
    "ds_read_b64_tr_b16 a[92:93], v5 offset:61440\n\t"
    "v_mfma_f32_16x16x32_bf16 v[180:183], a[76:79], a[212:215], v[180:183]\n\t"
    "v_mfma_f32_16x16x32_bf16 v[176:179], a[76:79], a[208:211], v[176:179]\n\t"
    "ds_read_b64_tr_b16 a[94:95], v5 offset:61952\n\t"
    "v_mfma_f32_16x16x32_bf16 v[176:179], a[72:75], a[192:195], v[176:179]\n\t"
    "s_waitcnt lgkmcnt(4)\n\t"
    "v_mfma_f32_16x16x32_bf16 v[176:179], a[80:83], a[0:3], v[176:179]\n\t"
    "ds_read_b64_tr_b16 a[64:65], v6 offset:16384\n\t"
    "v_mfma_f32_16x16x32_bf16 v[176:179], a[84:87], a[16:19], v[176:179]\n\t"
    "v_mfma_f32_16x16x32_bf16 v[180:183], a[84:87], a[20:23], v[180:183]\n\t"
    "ds_read_b64_tr_b16 a[66:67], v6 offset:16896\n\t"
    "v_mfma_f32_16x16x32_bf16 v[180:183], a[80:83], a[4:7], v[180:183]\n\t"
    "v_mfma_f32_16x16x32_bf16 v[184:187], a[80:83], a[8:11], v[184:187]\n\t"
    "ds_read_b64_tr_b16 a[68:69], v6 offset:20480\n\t"
    "v_mfma_f32_16x16x32_bf16 v[184:187], a[84:87], a[24:27], v[184:187]\n\t"
    "s_waitcnt vmcnt(2)\n\t"
    "buffer_store_dwordx2 v[232:233], v18, s[44:47], 0 offen offset:128 sc1\n\t"
    "v_mfma_f32_16x16x32_bf16 v[188:191], a[84:87], a[28:31], v[188:191]\n\t"
    "ds_read_b64_tr_b16 a[70:71], v6 offset:20992\n\t"
    "v_mfma_f32_16x16x32_bf16 v[188:191], a[80:83], a[12:15], v[188:191]\n\t"
    "s_waitcnt lgkmcnt(4)\n\t"
    "v_mfma_f32_16x16x32_bf16 v[188:191], a[88:91], a[44:47], v[188:191]\n\t"
    "ds_read_b64_tr_b16 a[72:73], v6 offset:24576\n\t"
    "v_mfma_f32_16x16x32_bf16 v[188:191], a[92:95], a[60:63], v[188:191]\n\t"
    "v_mfma_f32_16x16x32_bf16 v[184:187], a[92:95], a[56:59], v[184:187]\n\t"
    "ds_read_b64_tr_b16 a[74:75], v6 offset:25088\n\t"
    "v_mfma_f32_16x16x32_bf16 v[184:187], a[88:91], a[40:43], v[184:187]\n\t"
    "v_mfma_f32_16x16x32_bf16 v[180:183], a[88:91], a[36:39], v[180:183]\n\t"
    "ds_read_b64_tr_b16 a[76:77], v6 offset:28672\n\t"
    "v_mfma_f32_16x16x32_bf16 v[180:183], a[92:95], a[52:55], v[180:183]\n\t"
    "v_mfma_f32_16x16x32_bf16 v[176:179], a[92:95], a[48:51], v[176:179]\n\t"
    "ds_read_b64_tr_b16 a[78:79], v6 offset:29184\n\t"
    "v_mfma_f32_16x16x32_bf16 v[176:179], a[88:91], a[32:35], v[176:179]\n\t"
    "s_waitcnt lgkmcnt(4)\n\t"
    "v_mfma_f32_16x16x32_bf16 v[192:195], a[64:67], a[160:163], v[192:195]\n\t"
    "ds_read_b64_tr_b16 a[80:81], v6 offset:49152\n\t"
    "v_mfma_f32_16x16x32_bf16 v[192:195], a[68:71], a[176:179], v[192:195]\n\t"
    "v_mfma_f32_16x16x32_bf16 v[196:199], a[68:71], a[180:183], v[196:199]\n\t"
    "ds_read_b64_tr_b16 a[82:83], v6 offset:49664\n\t"
    "v_mfma_f32_16x16x32_bf16 v[196:199], a[64:67], a[164:167], v[196:199]\n\t"
    "v_mfma_f32_16x16x32_bf16 v[200:203], a[64:67], a[168:171], v[200:203]\n\t"
    "ds_read_b64_tr_b16 a[84:85], v6 offset:53248\n\t"
    "v_mfma_f32_16x16x32_bf16 v[200:203], a[68:71], a[184:187], v[200:203]\n\t"
    "v_cvt_pk_bf16_f32 v176, v176, v177\n\t"
    "v_cvt_pk_bf16_f32 v177, v178, v179\n\t"
    "v_mfma_f32_16x16x32_bf16 v[204:207], a[68:71], a[188:191], v[204:207]\n\t"
    "v_cvt_pk_bf16_f32 v178, v180, v181\n\t"
    "v_cvt_pk_bf16_f32 v179, v182, v183\n\t"
    "ds_read_b64_tr_b16 a[86:87], v6 offset:53760\n\t"
    "v_mfma_f32_16x16x32_bf16 v[204:207], a[64:67], a[172:175], v[204:207]\n\t"
    "v_cvt_pk_bf16_f32 v180, v184, v185\n\t"
    "v_cvt_pk_bf16_f32 v181, v186, v187\n\t"
    "s_waitcnt lgkmcnt(4)\n\t"
    "v_mfma_f32_16x16x32_bf16 v[204:207], a[72:75], a[204:207], v[204:207]\n\t"
    "v_cvt_pk_bf16_f32 v182, v188, v189\n\t"
    "v_cvt_pk_bf16_f32 v183, v190, v191\n\t"
    "ds_read_b64_tr_b16 a[88:89], v6 offset:57344\n\t"
    "v_mfma_f32_16x16x32_bf16 v[204:207], a[76:79], a[220:223], v[204:207]\n\t"
    "buffer_store_dwordx4 v[176:179], v16, s[44:47], s80 offen offset:2048 sc1\n\t"
    "v_mfma_f32_16x16x32_bf16 v[200:203], a[76:79], a[216:219], v[200:203]\n\t"
    "ds_read_b64_tr_b16 a[90:91], v6 offset:57856\n\t"
    "v_mfma_f32_16x16x32_bf16 v[200:203], a[72:75], a[200:203], v[200:203]\n\t"
    "buffer_store_dwordx4 v[180:183], v16, s[44:47], s80 offen offset:3072 sc1\n\t"
    "v_mfma_f32_16x16x32_bf16 v[196:199], a[72:75], a[196:199], v[196:199]\n\t"
    "ds_read_b64_tr_b16 a[92:93], v6 offset:61440\n\t"
    "v_mfma_f32_16x16x32_bf16 v[196:199], a[76:79], a[212:215], v[196:199]\n\t"
    "v_mfma_f32_16x16x32_bf16 v[192:195], a[76:79], a[208:211], v[192:195]\n\t"
    "ds_read_b64_tr_b16 a[94:95], v6 offset:61952\n\t"
    "v_mfma_f32_16x16x32_bf16 v[192:195], a[72:75], a[192:195], v[192:195]\n\t"
    "s_waitcnt lgkmcnt(4)\n\t"
    "v_mfma_f32_16x16x32_bf16 v[192:195], a[80:83], a[0:3], v[192:195]\n\t"
    "ds_read_b64_tr_b16 a[64:65], v7 offset:16384\n\t"
    "v_mfma_f32_16x16x32_bf16 v[192:195], a[84:87], a[16:19], v[192:195]\n\t"
    "v_mfma_f32_16x16x32_bf16 v[196:199], a[84:87], a[20:23], v[196:199]\n\t"
    "ds_read_b64_tr_b16 a[66:67], v7 offset:16896\n\t"
    "v_mfma_f32_16x16x32_bf16 v[196:199], a[80:83], a[4:7], v[196:199]\n\t"
    "v_mfma_f32_16x16x32_bf16 v[200:203], a[80:83], a[8:11], v[200:203]\n\t"
    "ds_read_b64_tr_b16 a[68:69], v7 offset:20480\n\t"
    "v_mfma_f32_16x16x32_bf16 v[200:203], a[84:87], a[24:27], v[200:203]\n\t"
    "v_mfma_f32_16x16x32_bf16 v[204:207], a[84:87], a[28:31], v[204:207]\n\t"
    "ds_read_b64_tr_b16 a[70:71], v7 offset:20992\n\t"
    "v_mfma_f32_16x16x32_bf16 v[204:207], a[80:83], a[12:15], v[204:207]\n\t"
    "s_waitcnt lgkmcnt(4)\n\t"
    "v_mfma_f32_16x16x32_bf16 v[204:207], a[88:91], a[44:47], v[204:207]\n\t"
    "ds_read_b64_tr_b16 a[72:73], v7 offset:24576\n\t"
    "v_mfma_f32_16x16x32_bf16 v[204:207], a[92:95], a[60:63], v[204:207]\n\t"
    "v_mfma_f32_16x16x32_bf16 v[200:203], a[92:95], a[56:59], v[200:203]\n\t"
    "ds_read_b64_tr_b16 a[74:75], v7 offset:25088\n\t"
    "v_mfma_f32_16x16x32_bf16 v[200:203], a[88:91], a[40:43], v[200:203]\n\t"
    "v_mfma_f32_16x16x32_bf16 v[196:199], a[88:91], a[36:39], v[196:199]\n\t"
    "ds_read_b64_tr_b16 a[76:77], v7 offset:28672\n\t"
    "v_mfma_f32_16x16x32_bf16 v[196:199], a[92:95], a[52:55], v[196:199]\n\t"
    "v_mfma_f32_16x16x32_bf16 v[192:195], a[92:95], a[48:51], v[192:195]\n\t"
    "ds_read_b64_tr_b16 a[78:79], v7 offset:29184\n\t"
    "v_mfma_f32_16x16x32_bf16 v[192:195], a[88:91], a[32:35], v[192:195]\n\t"
    "s_add_u32 s80, s80, 16512\n\t"
    "s_waitcnt lgkmcnt(4)\n\t"
    "v_mfma_f32_16x16x32_bf16 v[208:211], a[64:67], a[160:163], v[208:211]\n\t"
    "ds_read_b64_tr_b16 a[80:81], v7 offset:49152\n\t"
    "v_mfma_f32_16x16x32_bf16 v[208:211], a[68:71], a[176:179], v[208:211]\n\t"
    "v_mfma_f32_16x16x32_bf16 v[212:215], a[68:71], a[180:183], v[212:215]\n\t"
    "ds_read_b64_tr_b16 a[82:83], v7 offset:49664\n\t"
    "v_mfma_f32_16x16x32_bf16 v[212:215], a[64:67], a[164:167], v[212:215]\n\t"
    "v_mfma_f32_16x16x32_bf16 v[216:219], a[64:67], a[168:171], v[216:219]\n\t"
    "ds_read_b64_tr_b16 a[84:85], v7 offset:53248\n\t"
    "v_mfma_f32_16x16x32_bf16 v[216:219], a[68:71], a[184:187], v[216:219]\n\t"
    "v_cvt_pk_bf16_f32 v192, v192, v193\n\t"
    "v_cvt_pk_bf16_f32 v193, v194, v195\n\t"
    "v_mfma_f32_16x16x32_bf16 v[220:223], a[68:71], a[188:191], v[220:223]\n\t"
    "v_cvt_pk_bf16_f32 v194, v196, v197\n\t"
    "v_cvt_pk_bf16_f32 v195, v198, v199\n\t"
    "ds_read_b64_tr_b16 a[86:87], v7 offset:53760\n\t"
    "v_mfma_f32_16x16x32_bf16 v[220:223], a[64:67], a[172:175], v[220:223]\n\t"
    "v_cvt_pk_bf16_f32 v196, v200, v201\n\t"
    "v_cvt_pk_bf16_f32 v197, v202, v203\n\t"
    "s_waitcnt lgkmcnt(4)\n\t"
    "v_mfma_f32_16x16x32_bf16 v[220:223], a[72:75], a[204:207], v[220:223]\n\t"
    "v_cvt_pk_bf16_f32 v198, v204, v205\n\t"
    "v_cvt_pk_bf16_f32 v199, v206, v207\n\t"
    "ds_read_b64_tr_b16 a[88:89], v7 offset:57344\n\t"
    "v_mfma_f32_16x16x32_bf16 v[220:223], a[76:79], a[220:223], v[220:223]\n\t"
    "buffer_store_dwordx4 v[192:195], v16, s[44:47], s80 offen offset:0 sc1\n\t"
    "v_mfma_f32_16x16x32_bf16 v[216:219], a[76:79], a[216:219], v[216:219]\n\t"
    "ds_read_b64_tr_b16 a[90:91], v7 offset:57856\n\t"
    "v_mfma_f32_16x16x32_bf16 v[216:219], a[72:75], a[200:203], v[216:219]\n\t"
    "buffer_store_dwordx4 v[196:199], v16, s[44:47], s80 offen offset:1024 sc1\n\t"
    "v_mfma_f32_16x16x32_bf16 v[212:215], a[72:75], a[196:199], v[212:215]\n\t"
    "ds_read_b64_tr_b16 a[92:93], v7 offset:61440\n\t"
    "v_mfma_f32_16x16x32_bf16 v[212:215], a[76:79], a[212:215], v[212:215]\n\t"
    "v_mfma_f32_16x16x32_bf16 v[208:211], a[76:79], a[208:211], v[208:211]\n\t"
    "ds_read_b64_tr_b16 a[94:95], v7 offset:61952\n\t"
    "v_mfma_f32_16x16x32_bf16 v[208:211], a[72:75], a[192:195], v[208:211]\n\t"
    "s_waitcnt lgkmcnt(4)\n\t"
    "v_mfma_f32_16x16x32_bf16 v[208:211], a[80:83], a[0:3], v[208:211]\n\t"
    "ds_read_b64_tr_b16 a[64:65], v12 offset:0\n\t"
    "v_mfma_f32_16x16x32_bf16 v[208:211], a[84:87], a[16:19], v[208:211]\n\t"
    "v_mfma_f32_16x16x32_bf16 v[212:215], a[84:87], a[20:23], v[212:215]\n\t"
    "ds_read_b64_tr_b16 a[66:67], v12 offset:512\n\t"
    "v_mfma_f32_16x16x32_bf16 v[212:215], a[80:83], a[4:7], v[212:215]\n\t"
    "v_mfma_f32_16x16x32_bf16 v[216:219], a[80:83], a[8:11], v[216:219]\n\t"
    "ds_read_b64_tr_b16 a[68:69], v12 offset:4096\n\t"
    "v_mfma_f32_16x16x32_bf16 v[216:219], a[84:87], a[24:27], v[216:219]\n\t"
    "s_waitcnt vmcnt(2)\n\t"
    "buffer_store_dwordx2 v[232:233], v18, s[44:47], 0 offen offset:256 sc1\n\t"
    "v_mfma_f32_16x16x32_bf16 v[220:223], a[84:87], a[28:31], v[220:223]\n\t"
    "ds_read_b64_tr_b16 a[70:71], v12 offset:4608\n\t"
    "v_mfma_f32_16x16x32_bf16 v[220:223], a[80:83], a[12:15], v[220:223]\n\t"
    "s_waitcnt lgkmcnt(4)\n\t"
    "v_mfma_f32_16x16x32_bf16 v[220:223], a[88:91], a[44:47], v[220:223]\n\t"
    "ds_read_b64_tr_b16 a[72:73], v13 offset:0\n\t"
    "v_mfma_f32_16x16x32_bf16 v[220:223], a[92:95], a[60:63], v[220:223]\n\t"
    "v_mfma_f32_16x16x32_bf16 v[216:219], a[92:95], a[56:59], v[216:219]\n\t"
    "ds_read_b64_tr_b16 a[74:75], v13 offset:512\n\t"
    "v_mfma_f32_16x16x32_bf16 v[216:219], a[88:91], a[40:43], v[216:219]\n\t"
    "v_mfma_f32_16x16x32_bf16 v[212:215], a[88:91], a[36:39], v[212:215]\n\t"
    "ds_read_b64_tr_b16 a[76:77], v13 offset:4096\n\t"
    "v_mfma_f32_16x16x32_bf16 v[212:215], a[92:95], a[52:55], v[212:215]\n\t"
    "v_mfma_f32_16x16x32_bf16 v[208:211], a[92:95], a[48:51], v[208:211]\n\t"
    "ds_read_b64_tr_b16 a[78:79], v13 offset:4608\n\t"
    "v_mfma_f32_16x16x32_bf16 v[208:211], a[88:91], a[32:35], v[208:211]\n\t"
    "s_waitcnt lgkmcnt(4)\n\t"
    "v_mfma_f32_16x16x32_bf16 v[96:99], a[64:67], v[32:35], v[96:99]\n\t"
    "ds_read_b64_tr_b16 a[80:81], v12 offset:8192\n\t"
    "v_mfma_f32_16x16x32_bf16 v[96:99], a[68:71], v[48:51], v[96:99]\n\t"
    "v_mfma_f32_16x16x32_bf16 v[100:103], a[68:71], v[52:55], v[100:103]\n\t"
    "ds_read_b64_tr_b16 a[82:83], v12 offset:8704\n\t"
    "v_mfma_f32_16x16x32_bf16 v[100:103], a[64:67], v[36:39], v[100:103]\n\t"
    "v_mfma_f32_16x16x32_bf16 v[104:107], a[64:67], v[40:43], v[104:107]\n\t"
    "ds_read_b64_tr_b16 a[84:85], v12 offset:12288\n\t"
    "v_mfma_f32_16x16x32_bf16 v[104:107], a[68:71], v[56:59], v[104:107]\n\t"
    "v_cvt_pk_bf16_f32 v208, v208, v209\n\t"
    "v_cvt_pk_bf16_f32 v209, v210, v211\n\t"
    "v_mfma_f32_16x16x32_bf16 v[108:111], a[68:71], v[60:63], v[108:111]\n\t"
    "v_cvt_pk_bf16_f32 v210, v212, v213\n\t"
    "v_cvt_pk_bf16_f32 v211, v214, v215\n\t"
    "ds_read_b64_tr_b16 a[86:87], v12 offset:12800\n\t"
    "v_mfma_f32_16x16x32_bf16 v[108:111], a[64:67], v[44:47], v[108:111]\n\t"
    "v_cvt_pk_bf16_f32 v212, v216, v217\n\t"
    "v_cvt_pk_bf16_f32 v213, v218, v219\n\t"
    "s_waitcnt lgkmcnt(4)\n\t"
    "v_mfma_f32_16x16x32_bf16 v[124:127], a[72:75], v[44:47], v[124:127]\n\t"
    "v_cvt_pk_bf16_f32 v214, v220, v221\n\t"
    "v_cvt_pk_bf16_f32 v215, v222, v223\n\t"
    "ds_read_b64_tr_b16 a[88:89], v13 offset:8192\n\t"
    "v_mfma_f32_16x16x32_bf16 v[124:127], a[76:79], v[60:63], v[124:127]\n\t"
    "buffer_store_dwordx4 v[208:211], v16, s[44:47], s80 offen offset:2048 sc1\n\t"
    "v_mfma_f32_16x16x32_bf16 v[120:123], a[76:79], v[56:59], v[120:123]\n\t"
    "ds_read_b64_tr_b16 a[90:91], v13 offset:8704\n\t"
    "v_mfma_f32_16x16x32_bf16 v[120:123], a[72:75], v[40:43], v[120:123]\n\t"
    "buffer_store_dwordx4 v[212:215], v16, s[44:47], s80 offen offset:3072 sc1\n\t"
    "v_mfma_f32_16x16x32_bf16 v[116:119], a[72:75], v[36:39], v[116:119]\n\t"
    "ds_read_b64_tr_b16 a[92:93], v13 offset:12288\n\t"
    "v_mfma_f32_16x16x32_bf16 v[116:119], a[76:79], v[52:55], v[116:119]\n\t"
    "v_mfma_f32_16x16x32_bf16 v[112:115], a[76:79], v[48:51], v[112:115]\n\t"
    "ds_read_b64_tr_b16 a[94:95], v13 offset:12800\n\t"
    "v_mfma_f32_16x16x32_bf16 v[112:115], a[72:75], v[32:35], v[112:115]\n\t"
    "s_waitcnt lgkmcnt(4)\n\t"
    "v_mfma_f32_16x16x32_bf16 v[96:99], a[80:83], v[64:67], v[96:99]\n\t"
    "ds_read_b64_tr_b16 a[64:65], v8 offset:0\n\t"
    "v_mfma_f32_16x16x32_bf16 v[96:99], a[84:87], v[80:83], v[96:99]\n\t"
    "v_mfma_f32_16x16x32_bf16 v[100:103], a[84:87], v[84:87], v[100:103]\n\t"
    "ds_read_b64_tr_b16 a[66:67], v8 offset:512\n\t"
    "v_mfma_f32_16x16x32_bf16 v[100:103], a[80:83], v[68:71], v[100:103]\n\t"
    "v_mfma_f32_16x16x32_bf16 v[104:107], a[80:83], v[72:75], v[104:107]\n\t"
    "ds_read_b64_tr_b16 a[68:69], v8 offset:4096\n\t"
    "v_mfma_f32_16x16x32_bf16 v[104:107], a[84:87], v[88:91], v[104:107]\n\t"
    "buffer_load_dwordx2 v[234:235], v19, s[44:47], 0 offen offset:0 sc1\n\t"
    "v_mfma_f32_16x16x32_bf16 v[108:111], a[84:87], v[92:95], v[108:111]\n\t"
    "ds_read_b64_tr_b16 a[70:71], v8 offset:4608\n\t"
    "v_mfma_f32_16x16x32_bf16 v[108:111], a[80:83], v[76:79], v[108:111]\n\t"
    "s_waitcnt lgkmcnt(4)\n\t"
    "v_mfma_f32_16x16x32_bf16 v[124:127], a[88:91], v[76:79], v[124:127]\n\t"
    "ds_read_b64_tr_b16 a[72:73], v9 offset:0\n\t"
    "v_mfma_f32_16x16x32_bf16 v[124:127], a[92:95], v[92:95], v[124:127]\n\t"
    "v_mfma_f32_16x16x32_bf16 v[120:123], a[92:95], v[88:91], v[120:123]\n\t"
    "ds_read_b64_tr_b16 a[74:75], v9 offset:512\n\t"
    "v_mfma_f32_16x16x32_bf16 v[120:123], a[88:91], v[72:75], v[120:123]\n\t"
    "v_mfma_f32_16x16x32_bf16 v[116:119], a[88:91], v[68:71], v[116:119]\n\t"
    "ds_read_b64_tr_b16 a[76:77], v9 offset:4096\n\t"
    "v_mfma_f32_16x16x32_bf16 v[116:119], a[92:95], v[84:87], v[116:119]\n\t"
    "v_mfma_f32_16x16x32_bf16 v[112:115], a[92:95], v[80:83], v[112:115]\n\t"
    "ds_read_b64_tr_b16 a[78:79], v9 offset:4608\n\t"
    "v_mfma_f32_16x16x32_bf16 v[112:115], a[88:91], v[64:67], v[112:115]\n\t"
    "s_waitcnt lgkmcnt(4)\n\t"
    "v_mfma_f32_16x16x32_bf16 v[96:99], a[64:67], a[96:99], v[96:99]\n\t"
    "ds_read_b64_tr_b16 a[80:81], v8 offset:8192\n\t"
    "v_mfma_f32_16x16x32_bf16 v[96:99], a[68:71], a[112:115], v[96:99]\n\t"
    "v_mfma_f32_16x16x32_bf16 v[100:103], a[68:71], a[116:119], v[100:103]\n\t"
    "ds_read_b64_tr_b16 a[82:83], v8 offset:8704\n\t"
    "v_mfma_f32_16x16x32_bf16 v[100:103], a[64:67], a[100:103], v[100:103]\n\t"
    "v_mfma_f32_16x16x32_bf16 v[104:107], a[64:67], a[104:107], v[104:107]\n\t"
    "ds_read_b64_tr_b16 a[84:85], v8 offset:12288\n\t"
    "v_mfma_f32_16x16x32_bf16 v[104:107], a[68:71], a[120:123], v[104:107]\n\t"
    "v_mfma_f32_16x16x32_bf16 v[108:111], a[68:71], a[124:127], v[108:111]\n\t"
    "ds_read_b64_tr_b16 a[86:87], v8 offset:12800\n\t"
    "v_mfma_f32_16x16x32_bf16 v[108:111], a[64:67], a[108:111], v[108:111]\n\t"
    "s_waitcnt lgkmcnt(4)\n\t"
    "v_mfma_f32_16x16x32_bf16 v[124:127], a[72:75], a[108:111], v[124:127]\n\t"
    "ds_read_b64_tr_b16 a[88:89], v9 offset:8192\n\t"
    "v_mfma_f32_16x16x32_bf16 v[124:127], a[76:79], a[124:127], v[124:127]\n\t"
    "v_mfma_f32_16x16x32_bf16 v[120:123], a[76:79], a[120:123], v[120:123]\n\t"
    "ds_read_b64_tr_b16 a[90:91], v9 offset:8704\n\t"
    "v_mfma_f32_16x16x32_bf16 v[120:123], a[72:75], a[104:107], v[120:123]\n\t"
    "s_waitcnt vmcnt(1)\n\t"
    "buffer_store_dwordx2 v[232:233], v18, s[44:47], 0 offen offset:384 sc1\n\t"
    "v_mfma_f32_16x16x32_bf16 v[116:119], a[72:75], a[100:103], v[116:119]\n\t"
    "ds_read_b64_tr_b16 a[92:93], v9 offset:12288\n\t"
    "v_mfma_f32_16x16x32_bf16 v[116:119], a[76:79], a[116:119], v[116:119]\n\t"
    "v_mfma_f32_16x16x32_bf16 v[112:115], a[76:79], a[112:115], v[112:115]\n\t"
    "ds_read_b64_tr_b16 a[94:95], v9 offset:12800\n\t"
    "v_mfma_f32_16x16x32_bf16 v[112:115], a[72:75], a[96:99], v[112:115]\n\t"
    "s_waitcnt lgkmcnt(4)\n\t"
    "v_mfma_f32_16x16x32_bf16 v[96:99], a[80:83], a[128:131], v[96:99]\n\t"
    "ds_read_b64_tr_b16 a[64:65], v4 offset:0\n\t"
    "v_mfma_f32_16x16x32_bf16 v[96:99], a[84:87], a[144:147], v[96:99]\n\t"
    "s_waitcnt vmcnt(1)\n\t"
    "v_cmp_eq_u64 vcc, v[234:235], v[232:233]\n\t"
    "s_nop 0\n\t"
    "s_and_b32 s40, vcc_lo, 7\n\t"
    "s_andn2_b32 s40, s40, s7\n\t"
    "s_cbranch_scc0 XT0E_%=\n\t"
    "s_or_b32 s7, s7, s40\n\t"
    "s_bitcmp1_b32 s40, 0\n\t"
    "s_cbranch_scc0 XT01_%=\n\t"
    "buffer_load_dwordx4 v[136:139], v16, s[44:47], s4 offen offset:0 sc1\n\t"
    "buffer_load_dwordx4 v[140:143], v16, s[44:47], s4 offen offset:1024 sc1\n\t"
    "buffer_load_dwordx4 v[152:155], v16, s[44:47], s4 offen offset:2048 sc1\n\t"
    "buffer_load_dwordx4 v[156:159], v16, s[44:47], s4 offen offset:3072 sc1\n\t"
    "XT01_%=:\n\t"
    "s_bitcmp1_b32 s40, 1\n\t"
    "s_cbranch_scc0 XT02_%=\n\t"
    "buffer_load_dwordx4 v[168:171], v16, s[44:47], s5 offen offset:0 sc1\n\t"
    "buffer_load_dwordx4 v[172:175], v16, s[44:47], s5 offen offset:1024 sc1\n\t"
    "buffer_load_dwordx4 v[184:187], v16, s[44:47], s5 offen offset:2048 sc1\n\t"
    "buffer_load_dwordx4 v[188:191], v16, s[44:47], s5 offen offset:3072 sc1\n\t"
    "XT02_%=:\n\t"
    "s_bitcmp1_b32 s40, 2\n\t"
    "s_cbranch_scc0 XT03_%=\n\t"
    "buffer_load_dwordx4 v[200:203], v16, s[44:47], s6 offen offset:0 sc1\n\t"
    "buffer_load_dwordx4 v[204:207], v16, s[44:47], s6 offen offset:1024 sc1\n\t"
    "buffer_load_dwordx4 v[216:219], v16, s[44:47], s6 offen offset:2048 sc1\n\t"
    "buffer_load_dwordx4 v[220:223], v16, s[44:47], s6 offen offset:3072 sc1\n\t"
    "XT03_%=:\n\t"
    "XT0E_%=:\n\t"
    "v_mfma_f32_16x16x32_bf16 v[100:103], a[84:87], a[148:151], v[100:103]\n\t"
    "ds_read_b64_tr_b16 a[66:67], v4 offset:512\n\t"
    "v_mfma_f32_16x16x32_bf16 v[100:103], a[80:83], a[132:135], v[100:103]\n\t"
    "v_mfma_f32_16x16x32_bf16 v[104:107], a[80:83], a[136:139], v[104:107]\n\t"
    "ds_read_b64_tr_b16 a[68:69], v4 offset:4096\n\t"
    "v_mfma_f32_16x16x32_bf16 v[104:107], a[84:87], a[152:155], v[104:107]\n\t"
    "v_mfma_f32_16x16x32_bf16 v[108:111], a[84:87], a[156:159], v[108:111]\n\t"
    "ds_read_b64_tr_b16 a[70:71], v4 offset:4608\n\t"
    "v_mfma_f32_16x16x32_bf16 v[108:111], a[80:83], a[140:143], v[108:111]\n\t"
    "s_waitcnt lgkmcnt(4)\n\t"
    "v_mfma_f32_16x16x32_bf16 v[124:127], a[88:91], a[140:143], v[124:127]\n\t"
    "ds_read_b64_tr_b16 a[72:73], v5 offset:0\n\t"
    "v_mfma_f32_16x16x32_bf16 v[124:127], a[92:95], a[156:159], v[124:127]\n\t"
    "v_mfma_f32_16x16x32_bf16 v[120:123], a[92:95], a[152:155], v[120:123]\n\t"
    "ds_read_b64_tr_b16 a[74:75], v5 offset:512\n\t"
    "v_mfma_f32_16x16x32_bf16 v[120:123], a[88:91], a[136:139], v[120:123]\n\t"
    "v_mfma_f32_16x16x32_bf16 v[116:119], a[88:91], a[132:135], v[116:119]\n\t"
    "ds_read_b64_tr_b16 a[76:77], v5 offset:4096\n\t"
    "v_mfma_f32_16x16x32_bf16 v[116:119], a[92:95], a[148:151], v[116:119]\n\t"
    "v_mfma_f32_16x16x32_bf16 v[112:115], a[92:95], a[144:147], v[112:115]\n\t"
    "ds_read_b64_tr_b16 a[78:79], v5 offset:4608\n\t"
    "v_mfma_f32_16x16x32_bf16 v[112:115], a[88:91], a[128:131], v[112:115]\n\t"
    "s_waitcnt lgkmcnt(4)\n\t"
    "v_mfma_f32_16x16x32_bf16 v[96:99], a[64:67], a[160:163], v[96:99]\n\t"
    "ds_read_b64_tr_b16 a[80:81], v4 offset:8192\n\t"
    "v_mfma_f32_16x16x32_bf16 v[96:99], a[68:71], a[176:179], v[96:99]\n\t"
    "v_mfma_f32_16x16x32_bf16 v[100:103], a[68:71], a[180:183], v[100:103]\n\t"
    "ds_read_b64_tr_b16 a[82:83], v4 offset:8704\n\t"
    "v_mfma_f32_16x16x32_bf16 v[100:103], a[64:67], a[164:167], v[100:103]\n\t"
    "v_mfma_f32_16x16x32_bf16 v[104:107], a[64:67], a[168:171], v[104:107]\n\t"
    "ds_read_b64_tr_b16 a[84:85], v4 offset:12288\n\t"
    "v_mfma_f32_16x16x32_bf16 v[104:107], a[68:71], a[184:187], v[104:107]\n\t"
    "v_mfma_f32_16x16x32_bf16 v[108:111], a[68:71], a[188:191], v[108:111]\n\t"
    "ds_read_b64_tr_b16 a[86:87], v4 offset:12800\n\t"
    "v_mfma_f32_16x16x32_bf16 v[108:111], a[64:67], a[172:175], v[108:111]\n\t"
    "s_waitcnt lgkmcnt(4)\n\t"
    "v_mfma_f32_16x16x32_bf16 v[124:127], a[72:75], a[172:175], v[124:127]\n\t"
    "ds_read_b64_tr_b16 a[88:89], v5 offset:8192\n\t"
    "v_mfma_f32_16x16x32_bf16 v[124:127], a[76:79], a[188:191], v[124:127]\n\t"
    "v_mfma_f32_16x16x32_bf16 v[120:123], a[76:79], a[184:187], v[120:123]\n\t"
    "ds_read_b64_tr_b16 a[90:91], v5 offset:8704\n\t"
    "v_mfma_f32_16x16x32_bf16 v[120:123], a[72:75], a[168:171], v[120:123]\n\t"
    "v_mfma_f32_16x16x32_bf16 v[116:119], a[72:75], a[164:167], v[116:119]\n\t"
    "ds_read_b64_tr_b16 a[92:93], v5 offset:12288\n\t"
    "v_mfma_f32_16x16x32_bf16 v[116:119], a[76:79], a[180:183], v[116:119]\n\t"
    "v_mfma_f32_16x16x32_bf16 v[112:115], a[76:79], a[176:179], v[112:115]\n\t"
    "ds_read_b64_tr_b16 a[94:95], v5 offset:12800\n\t"
    "v_mfma_f32_16x16x32_bf16 v[112:115], a[72:75], a[160:163], v[112:115]\n\t"
    "s_waitcnt lgkmcnt(4)\n\t"
    "v_mfma_f32_16x16x32_bf16 v[96:99], a[80:83], a[192:195], v[96:99]\n\t"
    "ds_read_b64_tr_b16 a[64:65], v4 offset:32768\n\t"
    "v_mfma_f32_16x16x32_bf16 v[96:99], a[84:87], a[208:211], v[96:99]\n\t"
    "v_mfma_f32_16x16x32_bf16 v[100:103], a[84:87], a[212:215], v[100:103]\n\t"
    "ds_read_b64_tr_b16 a[66:67], v4 offset:33280\n\t"
    "v_mfma_f32_16x16x32_bf16 v[100:103], a[80:83], a[196:199], v[100:103]\n\t"
    "v_mfma_f32_16x16x32_bf16 v[104:107], a[80:83], a[200:203], v[104:107]\n\t"
    "ds_read_b64_tr_b16 a[68:69], v4 offset:36864\n\t"
    "v_mfma_f32_16x16x32_bf16 v[104:107], a[84:87], a[216:219], v[104:107]\n\t"
    "v_mfma_f32_16x16x32_bf16 v[108:111], a[84:87], a[220:223], v[108:111]\n\t"
    "ds_read_b64_tr_b16 a[70:71], v4 offset:37376\n\t"
    "v_mfma_f32_16x16x32_bf16 v[108:111], a[80:83], a[204:207], v[108:111]\n\t"
    "s_waitcnt lgkmcnt(4)\n\t"
    "v_mfma_f32_16x16x32_bf16 v[124:127], a[88:91], a[204:207], v[124:127]\n\t"
    "ds_read_b64_tr_b16 a[72:73], v5 offset:32768\n\t"
    "v_mfma_f32_16x16x32_bf16 v[124:127], a[92:95], a[220:223], v[124:127]\n\t"
    "v_mfma_f32_16x16x32_bf16 v[120:123], a[92:95], a[216:219], v[120:123]\n\t"
    "ds_read_b64_tr_b16 a[74:75], v5 offset:33280\n\t"
    "v_mfma_f32_16x16x32_bf16 v[120:123], a[88:91], a[200:203], v[120:123]\n\t"
    "v_mfma_f32_16x16x32_bf16 v[116:119], a[88:91], a[196:199], v[116:119]\n\t"
    "ds_read_b64_tr_b16 a[76:77], v5 offset:36864\n\t"
    "v_mfma_f32_16x16x32_bf16 v[116:119], a[92:95], a[212:215], v[116:119]\n\t"
    "v_mfma_f32_16x16x32_bf16 v[112:115], a[92:95], a[208:211], v[112:115]\n\t"
    "ds_read_b64_tr_b16 a[78:79], v5 offset:37376\n\t"
    "v_mfma_f32_16x16x32_bf16 v[112:115], a[88:91], a[192:195], v[112:115]\n\t"
    "s_waitcnt lgkmcnt(4)\n\t"
    "v_mfma_f32_16x16x32_bf16 v[96:99], a[64:67], a[0:3], v[96:99]\n\t"
    "ds_read_b64_tr_b16 a[80:81], v4 offset:40960\n\t"
    "v_mfma_f32_16x16x32_bf16 v[96:99], a[68:71], a[16:19], v[96:99]\n\t"
    "buffer_load_dwordx2 v[234:235], v19, s[44:47], 0 offen offset:0 sc1\n\t"
    "v_mfma_f32_16x16x32_bf16 v[100:103], a[68:71], a[20:23], v[100:103]\n\t"
    "ds_read_b64_tr_b16 a[82:83], v4 offset:41472\n\t"
    "v_mfma_f32_16x16x32_bf16 v[100:103], a[64:67], a[4:7], v[100:103]\n\t"
    "v_mfma_f32_16x16x32_bf16 v[104:107], a[64:67], a[8:11], v[104:107]\n\t"
    "ds_read_b64_tr_b16 a[84:85], v4 offset:45056\n\t"
    "v_mfma_f32_16x16x32_bf16 v[104:107], a[68:71], a[24:27], v[104:107]\n\t"
    "v_mfma_f32_16x16x32_bf16 v[108:111], a[68:71], a[28:31], v[108:111]\n\t"
    "ds_read_b64_tr_b16 a[86:87], v4 offset:45568\n\t"
    "v_mfma_f32_16x16x32_bf16 v[108:111], a[64:67], a[12:15], v[108:111]\n\t"
    "s_waitcnt lgkmcnt(4)\n\t"
    "v_mfma_f32_16x16x32_bf16 v[124:127], a[72:75], a[12:15], v[124:127]\n\t"
    "ds_read_b64_tr_b16 a[88:89], v5 offset:40960\n\t"
    "v_mfma_f32_16x16x32_bf16 v[124:127], a[76:79], a[28:31], v[124:127]\n\t"
    "v_mfma_f32_16x16x32_bf16 v[120:123], a[76:79], a[24:27], v[120:123]\n\t"
    "ds_read_b64_tr_b16 a[90:91], v5 offset:41472\n\t"
    "v_mfma_f32_16x16x32_bf16 v[120:123], a[72:75], a[8:11], v[120:123]\n\t"
    "v_mfma_f32_16x16x32_bf16 v[116:119], a[72:75], a[4:7], v[116:119]\n\t"
    "ds_read_b64_tr_b16 a[92:93], v5 offset:45056\n\t"
    "v_mfma_f32_16x16x32_bf16 v[116:119], a[76:79], a[20:23], v[116:119]\n\t"
    "v_mfma_f32_16x16x32_bf16 v[112:115], a[76:79], a[16:19], v[112:115]\n\t"
    "ds_read_b64_tr_b16 a[94:95], v5 offset:45568\n\t"
    "v_mfma_f32_16x16x32_bf16 v[112:115], a[72:75], a[0:3], v[112:115]\n\t"
    "s_waitcnt lgkmcnt(4)\n\t"
    "v_mfma_f32_16x16x32_bf16 v[96:99], a[80:83], a[32:35], v[96:99]\n\t"
    "v_mfma_f32_16x16x32_bf16 v[96:99], a[84:87], a[48:51], v[96:99]\n\t"
    "v_mfma_f32_16x16x32_bf16 v[100:103], a[84:87], a[52:55], v[100:103]\n\t"
    "v_mfma_f32_16x16x32_bf16 v[100:103], a[80:83], a[36:39], v[100:103]\n\t"
    "v_mfma_f32_16x16x32_bf16 v[104:107], a[80:83], a[40:43], v[104:107]\n\t"
    "v_mfma_f32_16x16x32_bf16 v[104:107], a[84:87], a[56:59], v[104:107]\n\t"
    "v_mfma_f32_16x16x32_bf16 v[108:111], a[84:87], a[60:63], v[108:111]\n\t"
    "v_mfma_f32_16x16x32_bf16 v[108:111], a[80:83], a[44:47], v[108:111]\n\t"
    "s_waitcnt lgkmcnt(0)\n\t"
    "v_mfma_f32_16x16x32_bf16 v[124:127], a[88:91], a[44:47], v[124:127]\n\t"
    "v_mfma_f32_16x16x32_bf16 v[124:127], a[92:95], a[60:63], v[124:127]\n\t"
    "v_mfma_f32_16x16x32_bf16 v[120:123], a[92:95], a[56:59], v[120:123]\n\t"
    "v_mfma_f32_16x16x32_bf16 v[120:123], a[88:91], a[40:43], v[120:123]\n\t"
    "v_mfma_f32_16x16x32_bf16 v[116:119], a[88:91], a[36:39], v[116:119]\n\t"
    "v_mfma_f32_16x16x32_bf16 v[116:119], a[92:95], a[52:55], v[116:119]\n\t"
    "v_mfma_f32_16x16x32_bf16 v[112:115], a[92:95], a[48:51], v[112:115]\n\t"
    "v_mfma_f32_16x16x32_bf16 v[112:115], a[88:91], a[32:35], v[112:115]\n\t"
    "s_cmp_eq_u32 s7, 7\n\t"
    "s_cbranch_scc1 XDONE_%=\n\t"
    "s_mov_b32 s83, 0x40000\n\t"
    "XLOOP_%=:\n\t"
    "s_waitcnt vmcnt(0)\n\t"
    "v_cmp_eq_u64 vcc, v[234:235], v[232:233]\n\t"
    "s_nop 0\n\t"
    "s_and_b32 s40, vcc_lo, 7\n\t"
    "s_andn2_b32 s40, s40, s7\n\t"
    "s_cbranch_scc0 XTLE_%=\n\t"
    "s_or_b32 s7, s7, s40\n\t"
    "s_bitcmp1_b32 s40, 0\n\t"
    "s_cbranch_scc0 XTL1_%=\n\t"
    "buffer_load_dwordx4 v[136:139], v16, s[44:47], s4 offen offset:0 sc1\n\t"
    "buffer_load_dwordx4 v[140:143], v16, s[44:47], s4 offen offset:1024 sc1\n\t"
    "buffer_load_dwordx4 v[152:155], v16, s[44:47], s4 offen offset:2048 sc1\n\t"
    "buffer_load_dwordx4 v[156:159], v16, s[44:47], s4 offen offset:3072 sc1\n\t"
    "XTL1_%=:\n\t"
    "s_bitcmp1_b32 s40, 1\n\t"
    "s_cbranch_scc0 XTL2_%=\n\t"
    "buffer_load_dwordx4 v[168:171], v16, s[44:47], s5 offen offset:0 sc1\n\t"
    "buffer_load_dwordx4 v[172:175], v16, s[44:47], s5 offen offset:1024 sc1\n\t"
    "buffer_load_dwordx4 v[184:187], v16, s[44:47], s5 offen offset:2048 sc1\n\t"
    "buffer_load_dwordx4 v[188:191], v16, s[44:47], s5 offen offset:3072 sc1\n\t"
    "XTL2_%=:\n\t"
    "s_bitcmp1_b32 s40, 2\n\t"
    "s_cbranch_scc0 XTL3_%=\n\t"
    "buffer_load_dwordx4 v[200:203], v16, s[44:47], s6 offen offset:0 sc1\n\t"
    "buffer_load_dwordx4 v[204:207], v16, s[44:47], s6 offen offset:1024 sc1\n\t"
    "buffer_load_dwordx4 v[216:219], v16, s[44:47], s6 offen offset:2048 sc1\n\t"
    "buffer_load_dwordx4 v[220:223], v16, s[44:47], s6 offen offset:3072 sc1\n\t"
    "XTL3_%=:\n\t"
    "XTLE_%=:\n\t"
    "s_cmp_eq_u32 s7, 7\n\t"
    "s_cbranch_scc1 XDONE_%=\n\t"
    "s_sleep 1\n\t"
    "buffer_load_dwordx2 v[234:235], v19, s[44:47], 0 offen offset:0 sc1\n\t"
    "s_sub_u32 s83, s83, 1\n\t"
    "s_cmp_lg_u32 s83, 0\n\t"
    "s_cbranch_scc1 XLOOP_%=\n\t"
    "XDONE_%=:\n\t"
    "s_waitcnt vmcnt(0)\n\t"
    "v_mfma_f32_16x16x32_bf16 v[96:99], v[24:27], v[136:139], v[96:99]\n\t"
    "v_mfma_f32_16x16x32_bf16 v[96:99], v[24:27], v[168:171], v[96:99]\n\t"
    "v_mfma_f32_16x16x32_bf16 v[96:99], v[24:27], v[200:203], v[96:99]\n\t"
    "v_mfma_f32_16x16x32_bf16 v[112:115], v[24:27], v[152:155], v[112:115]\n\t"
    "v_mfma_f32_16x16x32_bf16 v[112:115], v[24:27], v[184:187], v[112:115]\n\t"
    "v_mfma_f32_16x16x32_bf16 v[112:115], v[24:27], v[216:219], v[112:115]\n\t"
    "v_mfma_f32_16x16x32_bf16 v[100:103], v[22:25], v[136:139], v[100:103]\n\t"
    "v_mfma_f32_16x16x32_bf16 v[100:103], v[22:25], v[168:171], v[100:103]\n\t"
    "v_mfma_f32_16x16x32_bf16 v[100:103], v[22:25], v[200:203], v[100:103]\n\t"
    "v_mfma_f32_16x16x32_bf16 v[116:119], v[22:25], v[152:155], v[116:119]\n\t"
    "v_mfma_f32_16x16x32_bf16 v[116:119], v[22:25], v[184:187], v[116:119]\n\t"
    "v_mfma_f32_16x16x32_bf16 v[116:119], v[22:25], v[216:219], v[116:119]\n\t"
    "v_mfma_f32_16x16x32_bf16 v[104:107], v[24:27], v[140:143], v[104:107]\n\t"
    "v_mfma_f32_16x16x32_bf16 v[104:107], v[24:27], v[172:175], v[104:107]\n\t"
    "v_cvt_pk_bf16_f32 v128, v96, v97\n\t"
    "v_cvt_pk_bf16_f32 v130, v112, v113\n\t"
    "v_mfma_f32_16x16x32_bf16 v[104:107], v[24:27], v[204:207], v[104:107]\n\t"
    "v_cvt_pk_bf16_f32 v129, v98, v99\n\t"
    "v_cvt_pk_bf16_f32 v131, v114, v115\n\t"
    "v_mfma_f32_16x16x32_bf16 v[120:123], v[24:27], v[156:159], v[120:123]\n\t"
    "v_permlane16_swap_b32 v128, v130\n\t"
    "s_nop 0\n\t"
    "v_mfma_f32_16x16x32_bf16 v[120:123], v[24:27], v[188:191], v[120:123]\n\t"
    "v_permlane16_swap_b32 v129, v131\n\t"
    "buffer_store_dwordx4 v[128:131], v17, s[76:79], 0 offen sc1\n\t"
    "v_mfma_f32_16x16x32_bf16 v[120:123], v[24:27], v[220:223], v[120:123]\n\t"
    "v_mfma_f32_16x16x32_bf16 v[108:111], v[22:25], v[140:143], v[108:111]\n\t"
    "v_mfma_f32_16x16x32_bf16 v[108:111], v[22:25], v[172:175], v[108:111]\n\t"
    "v_cvt_pk_bf16_f32 v132, v100, v101\n\t"
    "v_cvt_pk_bf16_f32 v134, v116, v117\n\t"
    "v_mfma_f32_16x16x32_bf16 v[108:111], v[22:25], v[204:207], v[108:111]\n\t"
    "v_cvt_pk_bf16_f32 v133, v102, v103\n\t"
    "v_cvt_pk_bf16_f32 v135, v118, v119\n\t"
    "v_mfma_f32_16x16x32_bf16 v[124:127], v[22:25], v[156:159], v[124:127]\n\t"
    "v_permlane16_swap_b32 v132, v134\n\t"
    "s_nop 0\n\t"
    "v_mfma_f32_16x16x32_bf16 v[124:127], v[22:25], v[188:191], v[124:127]\n\t"
    "v_permlane16_swap_b32 v133, v135\n\t"
    "buffer_store_dwordx4 v[132:135], v17, s[76:79], s36 offen sc1\n\t"
    "v_mfma_f32_16x16x32_bf16 v[124:127], v[22:25], v[220:223], v[124:127]\n\t"
    "v_cvt_pk_bf16_f32 v144, v104, v105\n\t"
    "v_cvt_pk_bf16_f32 v146, v120, v121\n\t"
    "v_cvt_pk_bf16_f32 v145, v106, v107\n\t"
    "v_cvt_pk_bf16_f32 v147, v122, v123\n\t"
    "v_permlane16_swap_b32 v144, v146\n\t"
    "s_nop 0\n\t"
    "v_permlane16_swap_b32 v145, v147\n\t"
    "buffer_store_dwordx4 v[144:147], v17, s[76:79], s37 offen sc1\n\t"
    "s_nop 0\n\t"
    "v_cvt_pk_bf16_f32 v148, v108, v109\n\t"
    "v_cvt_pk_bf16_f32 v150, v124, v125\n\t"
    "v_cvt_pk_bf16_f32 v149, v110, v111\n\t"
    "v_cvt_pk_bf16_f32 v151, v126, v127\n\t"
    "v_permlane16_swap_b32 v148, v150\n\t"
    "s_nop 0\n\t"
    "v_permlane16_swap_b32 v149, v151\n\t"
    "buffer_store_dwordx4 v[148:151], v17, s[76:79], s38 offen sc1\n\t"
    "s_or_b32 s83, s2, s3\n\t"
    "s_cbranch_scc1 EPD_%=\n\t"
    "s_mov_b32 s83, 0x40000\n\t"
    "EPL_%=:\n\t"
    "v_cmp_ne_u64 s[84:85], v[240:241], v[232:233]\n\t"
    "s_nop 0\n\t"
    "s_or_b64 vcc, s[84:85], 0\n\t"
    "v_cmp_ne_u64 s[84:85], v[242:243], v[232:233]\n\t"
    "s_nop 0\n\t"
    "s_or_b64 vcc, s[84:85], vcc\n\t"
    "v_cmp_ne_u64 s[84:85], v[244:245], v[232:233]\n\t"
    "s_nop 0\n\t"
    "s_or_b64 vcc, s[84:85], vcc\n\t"
    "v_cmp_ne_u64 s[84:85], v[246:247], v[232:233]\n\t"
    "s_nop 0\n\t"
    "s_or_b64 vcc, s[84:85], vcc\n\t"
    "s_sub_u32 s83, s83, 1\n\t"
    "s_cmp_eq_u64 vcc, 0\n\t"
    "s_cbranch_scc1 EPO_%=\n\t"
    "s_sleep 1\n\t"
    "buffer_load_dwordx4 v[240:243], v248, s[44:47], s86 offen sc1\n\t"
    "buffer_load_dwordx4 v[244:247], v248, s[44:47], s86 offen offset:16 sc1\n\t"
    "s_waitcnt vmcnt(0)\n\t"
    "s_cmp_lg_u32 s83, 0\n\t"
    "s_cbranch_scc1 EPL_%=\n\t"
    "EPO_%=:\n\t"
    "v_mov_b32 v248, 33554432\n\t"
    "s_mov_b64 exec, 1\n\t"
    "buffer_store_dwordx2 v[232:233], v248, s[44:47], 0 offen sc1\n\t"
    "s_mov_b64 exec, -1\n\t"
    "EPD_%=:\n\t"
    "KEND_%=:\n\t"
    :: "{s[0:1]}"(__builtin_amdgcn_kernarg_segment_ptr()), "{s2}"(blockIdx.x), "{v0}"(threadIdx.x), "{s3}"((unsigned)(size_t)smem)
    : "memory", "scc", "vcc", "v1", "v2", "v3", "v4", "v5", "v6", "v7", "v8", "v9", "v10", "v11", "v12", "v13", "v14", "v15", "v16", "v17", "v18", "v19", "v20", "v21", "v22", "v23", "v24", "v25", "v26", "v27", "v28", "v29", "v30", "v31", "v32", "v33", "v34", "v35", "v36", "v37", "v38", "v39", "v40", "v41", "v42", "v43", "v44", "v45", "v46", "v47", "v48", "v49", "v50", "v51", "v52", "v53", "v54", "v55", "v56", "v57", "v58", "v59", "v60", "v61", "v62", "v63", "v64", "v65", "v66", "v67", "v68", "v69", "v70", "v71", "v72", "v73", "v74", "v75", "v76", "v77", "v78", "v79", "v80", "v81", "v82", "v83", "v84", "v85", "v86", "v87", "v88", "v89", "v90", "v91", "v92", "v93", "v94", "v95", "v96", "v97", "v98", "v99", "v100", "v101", "v102", "v103", "v104", "v105", "v106", "v107", "v108", "v109", "v110", "v111", "v112", "v113", "v114", "v115", "v116", "v117", "v118", "v119", "v120", "v121", "v122", "v123", "v124", "v125", "v126", "v127", "v128", "v129", "v130", "v131", "v132", "v133", "v134", "v135", "v136", "v137", "v138", "v139", "v140", "v141", "v142", "v143", "v144", "v145", "v146", "v147", "v148", "v149", "v150", "v151", "v152", "v153", "v154", "v155", "v156", "v157", "v158", "v159", "v160", "v161", "v162", "v163", "v164", "v165", "v166", "v167", "v168", "v169", "v170", "v171", "v172", "v173", "v174", "v175", "v176", "v177", "v178", "v179", "v180", "v181", "v182", "v183", "v184", "v185", "v186", "v187", "v188", "v189", "v190", "v191", "v192", "v193", "v194", "v195", "v196", "v197", "v198", "v199", "v200", "v201", "v202", "v203", "v204", "v205", "v206", "v207", "v208", "v209", "v210", "v211", "v212", "v213", "v214", "v215", "v216", "v217", "v218", "v219", "v220", "v221", "v222", "v223", "v224", "v225", "v226", "v227", "v228", "v229", "v230", "v231", "v232", "v233", "v234", "v235", "v236", "v237", "v238", "v239", "v240", "v241", "v242", "v243", "v244", "v245", "v246", "v247", "v248", "v249", "v250", "v251", "v252", "v253", "v254", "v255", "a0", "a1", "a2", "a3", "a4", "a5", "a6", "a7", "a8", "a9", "a10", "a11", "a12", "a13", "a14", "a15", "a16", "a17", "a18", "a19", "a20", "a21", "a22", "a23", "a24", "a25", "a26", "a27", "a28", "a29", "a30", "a31", "a32", "a33", "a34", "a35", "a36", "a37", "a38", "a39", "a40", "a41", "a42", "a43", "a44", "a45", "a46", "a47", "a48", "a49", "a50", "a51", "a52", "a53", "a54", "a55", "a56", "a57", "a58", "a59", "a60", "a61", "a62", "a63", "a64", "a65", "a66", "a67", "a68", "a69", "a70", "a71", "a72", "a73", "a74", "a75", "a76", "a77", "a78", "a79", "a80", "a81", "a82", "a83", "a84", "a85", "a86", "a87", "a88", "a89", "a90", "a91", "a92", "a93", "a94", "a95", "a96", "a97", "a98", "a99", "a100", "a101", "a102", "a103", "a104", "a105", "a106", "a107", "a108", "a109", "a110", "a111", "a112", "a113", "a114", "a115", "a116", "a117", "a118", "a119", "a120", "a121", "a122", "a123", "a124", "a125", "a126", "a127", "a128", "a129", "a130", "a131", "a132", "a133", "a134", "a135", "a136", "a137", "a138", "a139", "a140", "a141", "a142", "a143", "a144", "a145", "a146", "a147", "a148", "a149", "a150", "a151", "a152", "a153", "a154", "a155", "a156", "a157", "a158", "a159", "a160", "a161", "a162", "a163", "a164", "a165", "a166", "a167", "a168", "a169", "a170", "a171", "a172", "a173", "a174", "a175", "a176", "a177", "a178", "a179", "a180", "a181", "a182", "a183", "a184", "a185", "a186", "a187", "a188", "a189", "a190", "a191", "a192", "a193", "a194", "a195", "a196", "a197", "a198", "a199", "a200", "a201", "a202", "a203", "a204", "a205", "a206", "a207", "a208", "a209", "a210", "a211", "a212", "a213", "a214", "a215", "a216", "a217", "a218", "a219", "a220", "a221", "a222", "a223", "a224", "a225", "a226", "a227", "a228", "a229", "a230", "a231", "a232", "a233", "a234", "a235", "a236", "a237", "a238", "a239", "a240", "a241", "a242", "a243", "a244", "a245", "a246", "a247", "a248", "a249", "a250", "a251", "a252", "a253", "a254", "a255", "s4", "s5", "s6", "s7", "s8", "s9", "s10", "s11", "s12", "s13", "s14", "s15", "s16", "s17", "s18", "s19", "s20", "s21", "s22", "s23", "s24", "s25", "s26", "s27", "s28", "s29", "s30", "s31", "s33", "s34", "s35", "s36", "s37", "s38", "s39", "s40", "s41", "s42", "s43", "s44", "s45", "s46", "s47", "s48", "s49", "s50", "s51", "s52", "s53", "s54", "s55", "s56", "s57", "s58", "s59", "s60", "s61", "s62", "s63", "s64", "s65", "s66", "s67", "s68", "s69", "s70", "s71", "s72", "s73", "s74", "s75", "s76", "s77", "s78", "s79", "s80", "s81", "s82", "s83", "s84", "s85", "s86", "s87", "s88", "s89", "s90", "s91", "s92", "s93", "s94", "s95", "s96", "s97", "s98", "s99");
}

extern "C" void kernel_launch(void* const* d_in, const int* in_sizes, int n_in, void* d_out, int out_size, void* d_ws, size_t ws_size, hipStream_t stream) {
  swiglu_fused<<<256, 256, 0, stream>>>((const char*)d_in[0], (const char*)d_in[1], (const char*)d_in[2], (const char*)d_in[3], (char*)d_ws, (char*)d_out);
}
```
